```python
import torch

def _softmax(x, axis=-1):
    return torch.softmax(x, dim=axis)

def _gelu(x):
    return torch.nn.functional.gelu(x, approximate='tanh')

def _silu(x):
    return torch.nn.functional.silu(x)

def _sigmoid(x):
    return torch.sigmoid(x)

def _layernorm(x, axis=-1, eps=1e-05):
    m = x.mean(dim=axis, keepdim=True)
    v = x.var(dim=axis, keepdim=True, unbiased=False)
    return (x - m) / torch.sqrt(v + eps)

def _scatter_add(tgt, idx, src, axis=0):
    return tgt.clone().index_add_(axis, idx, src)

def _segment_sum(src, seg, n):
    out = torch.zeros((n,) + src.shape[1:], dtype=src.dtype)
    return out.index_add_(0, seg, src)

def _pad(x, pw):
    flat = tuple((v for pair in reversed(pw) for v in pair))
    return torch.nn.functional.pad(x, flat)
kernel_name = 'adaLN_cond__sliding_block_cached__dense_activation_squared_relu__decode'

def setup_inputs():

    _w = lambda a: (lambda t: t.to(torch.bfloat16) if t.is_floating_point() else t)(torch.as_tensor(a))

    def _A_setup():
        B, Sq, D = (4, 1, 512)
        Hq, Hkv, Sk, Dh = (32, 8, 8192, 128)
        assert Hq % Hkv == 0, (Hq, Hkv)
        return {'x': _w((lambda *s: torch.randn(*s, dtype=torch.bfloat16) * 0.05)(B, Sq, D)), 'k_cache': _w((lambda *s: torch.randn(*s, dtype=torch.bfloat16) * 3.2)(B, Hkv, Sk, Dh)), 'v_cache': _w((lambda *s: torch.randn(*s, dtype=torch.bfloat16) * 0.4)(B, Hkv, Sk, Dh)), 'wq': _w((lambda *s: torch.randn(*s, dtype=torch.bfloat16) * 3.2)(D, Hq * Dh) * D ** (-0.5)), 'wo': _w((lambda *s: torch.randn(*s, dtype=torch.bfloat16) * 0.4)(Hq * Dh, D) * (Hq * Dh) ** (-0.5))}
    _A_in = _A_setup()

    def _B_setup():
        B, S, D, F = (4, 1, 512, 1792)
        return {'x': _w((lambda *s: torch.randn(*s, dtype=torch.bfloat16) * 0.05)(B, S, D)), 'w1': _w((lambda *s: torch.randn(*s, dtype=torch.bfloat16) * 0.05)(D, F)), 'b1': _w((lambda *s: torch.randn(*s, dtype=torch.bfloat16) * 0.4)(F)), 'w2': _w((lambda *s: torch.randn(*s, dtype=torch.bfloat16) * 0.05)(F, D)), 'b2': _w((lambda *s: torch.randn(*s, dtype=torch.bfloat16) * 3.2)(D))}
    _B_in = _B_setup()
    return {'A_x': _A_in['x'], 'A_k_cache': _A_in['k_cache'], 'A_v_cache': _A_in['v_cache'], 'A_wq': _A_in['wq'], 'A_wo': _A_in['wo'], 'B_x': _B_in['x'], 'B_w1': _B_in['w1'], 'B_b1': _B_in['b1'], 'B_w2': _B_in['w2'], 'B_b2': _B_in['b2'], 'c': _w((lambda *s: torch.randn(*s, dtype=torch.bfloat16) * 0.1)(4, 512) * 8.0), 'w_mod': _w((lambda *s: torch.randn(*s, dtype=torch.bfloat16) * 0.1)(512, 6 * 512) * 512 ** (-0.5) * 8.0)}

def reference(A_x, A_k_cache, A_v_cache, A_wq, A_wo, B_x, B_w1, B_b1, B_w2, B_b2, c, w_mod):

    def _A_ref(x, k_cache, v_cache, wq, wo):
        B, Sq, D = x.shape
        Hq, Dh = (32, 128)
        Hkv, Sk = (k_cache.shape[1], k_cache.shape[2])
        W = min(4096, max(256, Sk // 4))
        rep = Hq // Hkv
        q = (lambda x, *d: x.permute(*d))((x @ wq).reshape(B, Sq, Hq, Dh), 0, 2, 1, 3)
        k = (lambda x, n, axis: torch.repeat_interleave(x, n, dim=axis))(k_cache[:, :, Sk - W:, :], rep, axis=1)
        v = (lambda x, n, axis: torch.repeat_interleave(x, n, dim=axis))(v_cache[:, :, Sk - W:, :], rep, axis=1)
        scores = torch.einsum('bhqd,bhkd->bhqk', q, k) * Dh ** (-0.5)
        p = _softmax(scores, axis=-1)
        o = torch.einsum('bhqk,bhkd->bhqd', p, v)
        return (lambda x, *d: x.permute(*d))(o, 0, 2, 1, 3).reshape(B, Sq, Hq * Dh) @ wo

    def _B_ref(x, w1, b1, w2, b2):
        h = torch.einsum('bsd,df->bsf', x, w1) + b1
        zero = torch.zeros_like(h)
        r = torch.where(h > zero, h, zero)
        h = r * r
        out = torch.einsum('bsf,fd->bsd', h, w2) + b2
        return out
    x = A_x
    mod = (_silu(c) @ w_mod).reshape(c.shape[0], 6, 1, -1)
    sa, ca, ga, sb, cb, gb = (mod[:, 0].clone(), mod[:, 1].clone(), mod[:, 2].clone(), mod[:, 3].clone(), mod[:, 4].clone(), mod[:, 5].clone())
    h = _layernorm(x, axis=-1) * (1 + ca) + sa
    x = x + ga * _A_ref(h, A_k_cache, A_v_cache, A_wq, A_wo)
    h = _layernorm(x, axis=-1) * (1 + cb) + sb
    x = x + gb * _B_ref(h, B_w1, B_b1, B_w2, B_b2)
    return x
```

```cpp
#include <hip/hip_runtime.h>
#include <hip/hip_bf16.h>
#include <stdint.h>

#define MIMIC 0

typedef unsigned short u16;
constexpr int B = 4, D = 512, HQ = 32, HKV = 8, SK = 8192, DH = 128, W = 2048, F = 1792;
constexpr int NSPLIT = 8, TK = W / NSPLIT;

__device__ __forceinline__ float bf2f(u16 v) { return __uint_as_float(((unsigned)v) << 16); }
__device__ __forceinline__ u16 f2bf(float f) { __hip_bfloat16 h = __float2bfloat16(f); return *reinterpret_cast<u16*>(&h); }
__device__ __forceinline__ float rb(float f) {
  return f;
}

__device__ __forceinline__ float wave_sum(float v) {
  for (int m = 32; m >= 1; m >>= 1) v += __shfl_xor(v, m);
  return v;
}
__device__ __forceinline__ float wave_max(float v) {
  for (int m = 32; m >= 1; m >>= 1) v = fmaxf(v, __shfl_xor(v, m));
  return v;
}

template <int K>
__device__ __forceinline__ void gemv_tile(const float* s_in  , const u16* w, int ldw, int col0, float* s_red  , float* s_out  ) {
  const int tid = threadIdx.x, cg = tid & 7, kg = tid >> 3;
  float acc[4][8];
  for (int b = 0; b < 4; b++) for (int j = 0; j < 8; j++) acc[b][j] = 0.f;
  for (int k = kg; k < K; k += 32) {
    uint4 v = *reinterpret_cast<const uint4*>(w + (size_t)k * ldw + col0 + cg * 8);
    float wf[8] = {__uint_as_float(v.x << 16), __uint_as_float(v.x & 0xffff0000u), __uint_as_float(v.y << 16), __uint_as_float(v.y & 0xffff0000u),
                   __uint_as_float(v.z << 16), __uint_as_float(v.z & 0xffff0000u), __uint_as_float(v.w << 16), __uint_as_float(v.w & 0xffff0000u)};
#pragma unroll
    for (int b = 0; b < 4; b++) { float a = s_in[b * K + k];
#pragma unroll
      for (int j = 0; j < 8; j++) acc[b][j] += a * wf[j]; }
  }
#pragma unroll
  for (int b = 0; b < 4; b++)
#pragma unroll
    for (int j = 0; j < 8; j++) s_red[(kg * 4 + b) * 64 + cg * 8 + j] = acc[b][j];
  __syncthreads();
  { int b = tid >> 6, c = tid & 63; float s = 0.f;
    for (int g = 0; g < 32; g++) s += s_red[(g * 4 + b) * 64 + c];
    s_out[b * 64 + c] = s; }
  __syncthreads();
}

__global__ void __launch_bounds__(256) k_mod(const u16* c, const u16* wmod, float* mod) {
  __shared__ float s_in[4 * 512]; __shared__ float s_red[32 * 4 * 64]; __shared__ float s_out[256];
  for (int i = threadIdx.x; i < 2048; i += 256) { float v = bf2f(c[i]); s_in[i] = rb(v / (1.f + __expf(-v))); }
  __syncthreads();
  gemv_tile<512>(s_in, wmod, 3072, blockIdx.x * 64, s_red, s_out);
  int b = threadIdx.x >> 6, cc = threadIdx.x & 63;
  mod[b * 3072 + blockIdx.x * 64 + cc] = rb(s_out[threadIdx.x]);
}

__device__ __forceinline__ void ln_mod(const float* s_x, const float* mod, int so, int co, float* s_in) {
  int b = threadIdx.x >> 6, lane = threadIdx.x & 63;
  float v[8]; float s = 0.f;
  for (int j = 0; j < 8; j++) { v[j] = s_x[b * 512 + lane + 64 * j]; s += v[j]; }
  s = wave_sum(s); float mean = s * (1.f / 512.f);
  float q = 0.f;
  for (int j = 0; j < 8; j++) { float d = v[j] - mean; q += d * d; }
  q = wave_sum(q); float var = rb(q * (1.f / 512.f)); float m = rb(mean);
  float den = rb(sqrtf(rb(var + 1e-5f)));
  for (int j = 0; j < 8; j++) { int d = lane + 64 * j;
    float n = rb(rb(v[j] - m) / den);
    float h = rb(rb(n * rb(1.f + mod[b * 3072 + co + d])) + mod[b * 3072 + so + d]);
    s_in[b * 512 + d] = h; }
}

__global__ void __launch_bounds__(256) k_q(const u16* x, const u16* wq, const float* mod, float* q) {
  __shared__ float s_x[2048]; __shared__ float s_in[4 * 512]; __shared__ float s_red[32 * 4 * 64]; __shared__ float s_out[256];
  for (int i = threadIdx.x; i < 2048; i += 256) s_x[i] = bf2f(x[i]);
  __syncthreads();
  ln_mod(s_x, mod, 0, 512, s_in);
  __syncthreads();
  gemv_tile<512>(s_in, wq, 4096, blockIdx.x * 64, s_red, s_out);
  int b = threadIdx.x >> 6, cc = threadIdx.x & 63;
  q[b * 4096 + blockIdx.x * 64 + cc] = rb(s_out[threadIdx.x]);
}

__global__ void __launch_bounds__(256) k_attn(const u16* kc, const u16* vc, const float* q, float* po, float* pml) {
  __shared__ float s_q[512]; __shared__ float s_p[4][256]; __shared__ float s_r[4][4]; __shared__ float s_o[16][4][128];
  int s = blockIdx.x, kvh = blockIdx.y, b = blockIdx.z, tid = threadIdx.x, lane = tid & 63, wave = tid >> 6;
  for (int i = tid; i < 512; i += 256) s_q[i] = q[b * 4096 + kvh * 512 + i];
  __syncthreads();
  const size_t base = ((size_t)(b * HKV + kvh) * SK + (SK - W) + s * TK) * DH;
  float sc[4] = {0, 0, 0, 0};
  const u16* kr = kc + base + (size_t)tid * DH;
#pragma unroll 2
  for (int c = 0; c < 16; c++) {
    uint4 v = *reinterpret_cast<const uint4*>(kr + c * 8);
    float kf[8] = {__uint_as_float(v.x << 16), __uint_as_float(v.x & 0xffff0000u), __uint_as_float(v.y << 16), __uint_as_float(v.y & 0xffff0000u),
                   __uint_as_float(v.z << 16), __uint_as_float(v.z & 0xffff0000u), __uint_as_float(v.w << 16), __uint_as_float(v.w & 0xffff0000u)};
#pragma unroll
    for (int h = 0; h < 4; h++)
#pragma unroll
      for (int j = 0; j < 8; j++) sc[h] += kf[j] * s_q[h * 128 + c * 8 + j];
  }
  float m[4];
#pragma unroll
  for (int h = 0; h < 4; h++) { sc[h] = rb(rb(sc[h]) * 0.08838834764831845f); float t = wave_max(sc[h]); if (lane == 0) s_r[wave][h] = t; }
  __syncthreads();
#pragma unroll
  for (int h = 0; h < 4; h++) m[h] = fmaxf(fmaxf(s_r[0][h], s_r[1][h]), fmaxf(s_r[2][h], s_r[3][h]));
  __syncthreads();
#pragma unroll
  for (int h = 0; h < 4; h++) { float p = __expf(sc[h] - m[h]); float t = wave_sum(p); if (lane == 0) s_r[wave][h] = t; s_p[h][tid] = rb(p); }
  __syncthreads();
  int kg = tid >> 4, c = tid & 15;
  float acc[4][8];
#pragma unroll
  for (int h = 0; h < 4; h++)
#pragma unroll
 for (int j = 0; j < 8; j++) acc[h][j] = 0.f;
#pragma unroll 4
  for (int i = 0; i < 16; i++) { int key = kg + 16 * i;
    uint4 v = *reinterpret_cast<const uint4*>(vc + base + (size_t)key * DH + c * 8);
    float vf[8] = {__uint_as_float(v.x << 16), __uint_as_float(v.x & 0xffff0000u), __uint_as_float(v.y << 16), __uint_as_float(v.y & 0xffff0000u),
                   __uint_as_float(v.z << 16), __uint_as_float(v.z & 0xffff0000u), __uint_as_float(v.w << 16), __uint_as_float(v.w & 0xffff0000u)};
#pragma unroll
    for (int h = 0; h < 4; h++) { float p = s_p[h][key];
#pragma unroll
      for (int j = 0; j < 8; j++) acc[h][j] += p * vf[j]; } }
#pragma unroll
  for (int h = 0; h < 4; h++)
#pragma unroll
 for (int j = 0; j < 8; j++) s_o[kg][h][c * 8 + j] = acc[h][j];
  __syncthreads();
  for (int i = tid; i < 512; i += 256) { int h = i >> 7, d = i & 127; float t = 0.f; for (int g = 0; g < 16; g++) t += s_o[g][h][d];
    po[((size_t)(b * HQ + kvh * 4 + h) * NSPLIT + s) * DH + d] = t; }
  if (tid < 4) { int h = tid; float l = s_r[0][h] + s_r[1][h] + s_r[2][h] + s_r[3][h];
    float mh = h == 0 ? m[0] : h == 1 ? m[1] : h == 2 ? m[2] : m[3];
    pml[((b * HQ + kvh * 4 + h) * NSPLIT + s) * 2] = mh; pml[((b * HQ + kvh * 4 + h) * NSPLIT + s) * 2 + 1] = l; }
}

__global__ void __launch_bounds__(256) k_wo(const float* po, const float* pml, const u16* wo, float* part) {
  __shared__ float s_in[4 * 512]; __shared__ float s_red[32 * 4 * 64]; __shared__ float s_out[256];
  int cb = blockIdx.x, ks = blockIdx.y;
  for (int i = threadIdx.x; i < 2048; i += 256) { int b = i >> 9, r = i & 511, h = ks * 4 + (r >> 7), d = r & 127;
    const float* ml = pml + (b * HQ + h) * NSPLIT * 2; float M = -INFINITY;
    for (int s = 0; s < NSPLIT; s++) M = fmaxf(M, ml[2 * s]);
    float L = 0.f, o = 0.f;
    for (int s = 0; s < NSPLIT; s++) { float w = __expf(ml[2 * s] - M); L += w * ml[2 * s + 1]; o += w * po[((size_t)(b * HQ + h) * NSPLIT + s) * DH + d]; }
    s_in[i] = rb(o / L); }
  __syncthreads();
  gemv_tile<512>(s_in, wo + (size_t)ks * 512 * 512, 512, cb * 64, s_red, s_out);
  int b = threadIdx.x >> 6, cc = threadIdx.x & 63;
  part[(ks * 4 + b) * 512 + cb * 64 + cc] = s_out[threadIdx.x];
}

__global__ void __launch_bounds__(256) k_w1(const u16* x, const float* part, const float* mod, const u16* w1, const u16* b1, float* h1, float* x1) {
  __shared__ float s_x[2048]; __shared__ float s_in[4 * 512]; __shared__ float s_red[32 * 4 * 64]; __shared__ float s_out[256];
  for (int i = threadIdx.x; i < 2048; i += 256) { int b = i >> 9, d = i & 511; float t = 0.f;
    for (int k = 0; k < 8; k++) t += part[(k * 4 + b) * 512 + d];
    float v = rb(bf2f(x[i]) + rb(mod[b * 3072 + 1024 + d] * rb(t))); s_x[i] = v; if (blockIdx.x == 0) x1[i] = v; }
  __syncthreads();
  ln_mod(s_x, mod, 1536, 2048, s_in);
  __syncthreads();
  gemv_tile<512>(s_in, w1, F, blockIdx.x * 64, s_red, s_out);
  int b = threadIdx.x >> 6, cc = threadIdx.x & 63, f = blockIdx.x * 64 + cc;
  float h = rb(rb(s_out[threadIdx.x]) + bf2f(b1[f])); h = h > 0.f ? h : 0.f;
  h1[b * F + f] = rb(h * h);
}

__global__ void __launch_bounds__(256) k_w2(const float* h1, const float* x1, const float* mod, const u16* w2, const u16* b2, u16* out) {
  __shared__ float s_in[4 * F]; __shared__ float s_red[32 * 4 * 64]; __shared__ float s_out[256];
  for (int i = threadIdx.x; i < 4 * F; i += 256) s_in[i] = h1[i];
  __syncthreads();
  gemv_tile<F>(s_in, w2, 512, blockIdx.x * 64, s_red, s_out);
  int b = threadIdx.x >> 6, cc = threadIdx.x & 63, d = blockIdx.x * 64 + cc;
  float o = rb(rb(s_out[threadIdx.x]) + bf2f(b2[d]));
  out[b * 512 + d] = f2bf(x1[b * 512 + d] + rb(mod[b * 3072 + 2560 + d] * o));
}

extern "C" void kernel_launch(void* const* d_in, const int* in_sizes, int n_in, void* d_out, int out_size, void* d_ws, size_t ws_size, hipStream_t stream) {
  const u16* x = (const u16*)d_in[0]; const u16* kc = (const u16*)d_in[1]; const u16* vc = (const u16*)d_in[2];
  const u16* wq = (const u16*)d_in[3]; const u16* wo = (const u16*)d_in[4];
  const u16* w1 = (const u16*)d_in[6]; const u16* b1 = (const u16*)d_in[7]; const u16* w2 = (const u16*)d_in[8]; const u16* b2 = (const u16*)d_in[9];
  const u16* c = (const u16*)d_in[10]; const u16* wmod = (const u16*)d_in[11];
  float* ws = (float*)d_ws;
  float* mod = ws; float* q = mod + 4 * 3072; float* po = q + 4 * 4096; float* pml = po + B * HQ * NSPLIT * DH; float* part = pml + B * HQ * NSPLIT * 2;
  float* h1 = part + 8 * 2048; float* x1 = h1 + 4 * F;
  k_mod<<<48, 256, 0, stream>>>(c, wmod, mod);
  k_q<<<64, 256, 0, stream>>>(x, wq, mod, q);
  k_attn<<<dim3(NSPLIT, HKV, B), 256, 0, stream>>>(kc, vc, q, po, pml);
  k_wo<<<dim3(8, 8), 256, 0, stream>>>(po, pml, wo, part);
  k_w1<<<28, 256, 0, stream>>>(x, part, mod, w1, b1, h1, x1);
  k_w2<<<8, 256, 0, stream>>>(h1, x1, mod, w2, b2, (u16*)d_out);
}
```

```cpp
#include <hip/hip_runtime.h>
#include <hip/hip_bf16.h>
#pragma clang diagnostic ignored "-Wint-to-pointer-cast"

typedef unsigned u32;
typedef unsigned short u16;
typedef u32 v4u __attribute__((ext_vector_type(4)));
typedef u32 v2u __attribute__((ext_vector_type(2)));
typedef int v4i __attribute__((ext_vector_type(4)));
typedef float v4f __attribute__((ext_vector_type(4)));
typedef float v2f __attribute__((ext_vector_type(2)));
typedef short v4s __attribute__((ext_vector_type(4)));
typedef __bf16 v8bf __attribute__((ext_vector_type(8)));
typedef __bf16 v2bf __attribute__((ext_vector_type(2)));
#define LAS __attribute__((address_space(3)))
#define GAS __attribute__((address_space(1)))
#define DEV __device__ __forceinline__
#define FENCE() asm volatile("" ::: "memory")
#define SCHED() __builtin_amdgcn_sched_barrier(0)
#define PIN(v) asm volatile("" : "+v"(v))
#define PIN4(a) asm volatile("" : "+v"(a[0]), "+v"(a[1]), "+v"(a[2]), "+v"(a[3]))
#define PIN8(a) asm volatile("" : "+v"(a[0]), "+v"(a[1]), "+v"(a[2]), "+v"(a[3]), "+v"(a[4]), "+v"(a[5]), "+v"(a[6]), "+v"(a[7]))

#define PACE_A 195
#define PACE_B 248
#define PACE_M 193
#define KSTART 200
#define PACE_L 290
#define F_DELAY 1150
#define HSLOT 9
#define HPOLL0 2700
#define Q_DELAY 1500
#define WIN 32

constexpr u32 WS_EPOCH = 0, WS_MODR = 49152, WS_X = 65536, WS_ML = 98304, WS_GC = 106496, WS_O = 131072, WS_Y = 393216, WS_H = 655360, WS_Q = 720896,
              WS_END = WS_Q + 524288;
constexpr u32 WS_STAMP = 1u << 21;
constexpr u32 L_K = 0, L_V = 65536, L_T2 = 114688, L_T1 = 131072, L_RM = 147456, L_HF = L_RM + 1024, L_RED = L_HF + 2048, L_TS = L_RED + 2048, L_PL = L_TS + 4096,
              L_QL = L_PL + 2048, QL_S = 288, L_MX = L_QL + 4 * QL_S, L_SM = L_MX + 64, L_LN = L_SM + 64, L_ST = L_LN + 64, L_FLAG = L_ST + 128, L_END = L_FLAG + 64;
static_assert(L_END + 128 <= 163840, "lds");
enum { F_K = 4, F_V, F_LATE, F_WQ, F_H };
constexpr int CODE_TAIL = 11000;
constexpr u32 SPIN_CAP = 1u << 16;
constexpr int SC1 = 16, NT = 2;

DEV u32 rtv() { unsigned long long t; asm volatile("s_memrealtime %0\n\ts_waitcnt lgkmcnt(0)" : "=s"(t)::"memory"); return (u32)t; }
DEV u32 cyc() { return rtv() << 4; }
#define STAMP(k)
#define STAMP0(k)

DEV float bflo(u32 v) { return __uint_as_float(v << 16); }
DEV float bfhi(u32 v) { return __uint_as_float(v & 0xffff0000u); }
DEV v2f unpk(u32 v) { return v2f{bflo(v), bfhi(v)}; }
DEV u32 pk2(v2f p) { return __builtin_bit_cast(u32, __builtin_convertvector(p, v2bf)); }
DEV u32 pk(float lo, float hi) { return pk2(v2f{lo, hi}); }
DEV float rb(float f) { return bflo(pk(f, f)); }
DEV v2f rb2(v2f p) { return unpk(pk2(p)); }
DEV float fmx(float a, float b) { float r; asm("v_max_f32 %0, %1, %2" : "=v"(r) : "v"(a), "v"(b)); return r; }
DEV float fmx3(float a, float b, float c) { float r; asm("v_max3_f32 %0, %1, %2, %3" : "=v"(r) : "v"(a), "v"(b), "v"(c)); return r; }
DEV float fexp(float a) { return __builtin_amdgcn_exp2f(a * 1.4426950408889634f); }
DEV u32 nosent(u32 v) { return v < 0xfffffffeu ? v : 0xfffffffeu; }
DEV v4f mfma(v4u a, v4u b, v4f c) { return __builtin_amdgcn_mfma_f32_16x16x32_bf16(__builtin_bit_cast(v8bf, a), __builtin_bit_cast(v8bf, b), c, 0, 0, 0); }
DEV v4f mfma0(v4u a, v4u b) { return mfma(a, b, v4f{0.f, 0.f, 0.f, 0.f}); }
DEV v2u trr(u32 addr) { return __builtin_bit_cast(v2u, __builtin_amdgcn_ds_read_tr16_b64_v4i16((LAS v4s*)addr)); }
DEV v4u tr2(u32 a0, u32 a1) { v2u a = trr(a0), b = trr(a1); return v4u{a.x, a.y, b.x, b.y}; }
DEV v4u lds4(u32 addr) { return *(LAS v4u*)addr; }
template <int C> DEV float dppf(float x) { return __builtin_bit_cast(float, __builtin_amdgcn_mov_dpp(__builtin_bit_cast(int, x), C, 0xf, 0xf, true)); }
template <int C> DEV u32 dppu(u32 x) { return (u32)__builtin_amdgcn_mov_dpp((int)x, C, 0xf, 0xf, true); }
template <int C> DEV v4u dpp4(v4u x) { return v4u{dppu<C>(x.x), dppu<C>(x.y), dppu<C>(x.z), dppu<C>(x.w)}; }
constexpr int QX1 = 0xB1, QX2 = 0x4E, ROR4 = 0x124, ROR8 = 0x128;
DEV float xg_sum(float x) {
  auto s = __builtin_amdgcn_permlane16_swap(__float_as_uint(x), __float_as_uint(x), false, false);
  x = __uint_as_float(s[0]) + __uint_as_float(s[1]);
  auto t = __builtin_amdgcn_permlane32_swap(__float_as_uint(x), __float_as_uint(x), false, false);
  return __uint_as_float(t[0]) + __uint_as_float(t[1]);
}
DEV float xg_max(float x) {
  auto s = __builtin_amdgcn_permlane16_swap(__float_as_uint(x), __float_as_uint(x), false, false);
  x = fmaxf(__uint_as_float(s[0]), __uint_as_float(s[1]));
  auto t = __builtin_amdgcn_permlane32_swap(__float_as_uint(x), __float_as_uint(x), false, false);
  return fmaxf(__uint_as_float(t[0]), __uint_as_float(t[1]));
}
DEV float quad_sum(float x) { x += dppf<QX1>(x); return x + dppf<QX2>(x); }
DEV float wave_sum(float x) { x = quad_sum(x); x += dppf<ROR4>(x); x += dppf<ROR8>(x); return xg_sum(x); }
DEV u32 umax4(v4u v) { return max(max(v.x, v.y), max(v.z, v.w)); }
DEV bool any_nan(float a, float b) { return __any(__builtin_isunordered(a, b)); }

DEV v4i mkrs(const void* p, u32 bytes) { v4i r; r.x = (int)(size_t)p; r.y = (int)((size_t)p >> 32) & 0xffff; r.z = (int)bytes; r.w = 0x00020000; return r; }
DEV void dma1(v4i rs, u32 voff, u32 soff, u32 lds) {
  asm volatile("s_mov_b32 m0, %3\n\ts_nop 0\n\tbuffer_load_dwordx4 %1, %0, %2 offen nt lds" ::"s"(rs), "v"(voff), "s"(soff), "s"(lds) : "memory");
}
DEV void dma4(v4i rs, u32 voff, u32 soff, u32 lds) {
  asm volatile("s_mov_b32 m0, %3\n\ts_nop 0\n\tbuffer_load_dwordx4 %1, %0, %2 offen nt lds\n\tbuffer_load_dwordx4 %1, %0, %2 offen offset:1024 nt lds\n\t"
               "buffer_load_dwordx4 %1, %0, %2 offen offset:2048 nt lds\n\tbuffer_load_dwordx4 %1, %0, %2 offen offset:3072 nt lds" ::"s"(rs), "v"(voff), "s"(soff), "s"(lds) : "memory");
}
#define VMWAIT(n) asm volatile("s_waitcnt vmcnt(%0)" ::"i"(n) : "memory")
DEV u32 flag_get(int f) { return __builtin_amdgcn_readfirstlane(*(volatile LAS u32*)(L_FLAG + 4 * f)); }
DEV void flag_set(int f, u32 v) { FENCE(); *(volatile LAS u32*)(L_FLAG + 4 * f) = v; FENCE(); }
DEV void flag_wait(int f, u32 v) { while (flag_get(f) < v) __builtin_amdgcn_s_sleep(1); FENCE(); }
DEV void flag_wait_hot(int f, u32 v) { while (flag_get(f) < v) {} FENCE(); }
DEV void stage_wait(u32 v, int lane) { while (!__all(*(volatile LAS u32*)(L_FLAG + 4 * (lane & 3)) >= v)) {} FENCE(); }
DEV void wg_barrier() { asm volatile("s_waitcnt lgkmcnt(0)\n\ts_barrier" ::: "memory"); }

DEV float dot_sets(v4u a, const v4u (&w)[4]) {
  const v4f d0 = mfma0(a, w[0]), d1 = mfma0(a, w[1]), d2 = mfma0(a, w[2]), d3 = mfma0(a, w[3]);
  return (d0[0] + d1[1]) + (d2[2] + d3[3]);
}
DEV v4f mfma4(v2u a, u32 b0, u32 b1, v4f c) {
  return __builtin_amdgcn_mfma_f32_4x4x4bf16_1k(__builtin_bit_cast(v4s, a), __builtin_bit_cast(v4s, v2u{b0, b1}), c, 0, 0, 0);
}
template <int STRIDE>
DEV void gather_issue(__amdgpu_buffer_rsrc_t rws, u32 voff, v4u (&r)[8]) {
#pragma unroll
  for (int k = 0; k < 8; k++) r[k] = __builtin_amdgcn_raw_buffer_load_b128(rws, voff + (STRIDE < 4096 ? (k & 3) * STRIDE : 0), STRIDE < 4096 ? (k >> 2) * 4 * STRIDE : k * STRIDE, SC1);
}
template <int STRIDE>
DEV void gather_sum(__amdgpu_buffer_rsrc_t rws, u32 voff, v4u (&r)[8], const v2u (&w)[8], v4f& a0, v4f& a1) {
  for (u32 spin = 0;; spin++) {
    a0 = v4f{0.f, 0.f, 0.f, 0.f}; a1 = a0;
#pragma unroll
    for (int k = 0; k < 8; k++) { a0 = mfma4(w[k], r[k].x, r[k].y, a0); a1 = mfma4(w[k], r[k].z, r[k].w, a1); }
    if (__builtin_expect(!any_nan(a0[0], a1[0]), 1)) break;
    bool any = false;
    FENCE();
#pragma unroll
    for (int k = 0; k < 8; k++)
      if (umax4(r[k]) == 0xffffffffu) { r[k] = __builtin_amdgcn_raw_buffer_load_b128(rws, voff + (STRIDE < 4096 ? (k & 3) * STRIDE : 0), STRIDE < 4096 ? (k >> 2) * 4 * STRIDE : k * STRIDE, SC1); any = true; }
    if (!__any(any) || spin >= SPIN_CAP) break;
  }
}
DEV void marks_wait(__amdgpu_buffer_rsrc_t rws, u32 voff, u32 tag) {
  for (u32 spin = 0; spin < SPIN_CAP; spin++) {
    FENCE();
    if (__all(__builtin_amdgcn_raw_buffer_load_b32(rws, voff, 0, SC1) == tag)) break;
  }
  FENCE();
}

__global__ void __launch_bounds__(320, 2)
fused_block(unsigned char* pws, const u16* __restrict__ pwq, const u16* __restrict__ pkc, const u16* __restrict__ pvc, const u16* __restrict__ px,
            const u16* __restrict__ pc, const u16* __restrict__ pwo, const u16* __restrict__ pw1, const u16* __restrict__ pwm, const u16* __restrict__ pb1,
            const u16* __restrict__ pw2, const u16* __restrict__ pb2, u16* __restrict__ pout) {
  __shared__ __attribute__((aligned(16))) char smem[163840];
  asm volatile("" ::"v"((LAS char*)smem));
  const int lane = threadIdx.x & 63, wave = __builtin_amdgcn_readfirstlane(threadIdx.x >> 6);
  const int wg = blockIdx.x, x = wg & 7, i = wg >> 3, b = i >> 3, s = i & 7;
  const int role = i & 3;
  const bool isM = role == 0, isO = role == 1 || role == 2, isF = role == 3 && x < 7;
  const int u = x * 8 + (i >> 2);
  const int oc = (i >> 2) * 2 + role - 1;

  if (__builtin_expect(wave == 4, 1)) {
    __builtin_amdgcn_s_setprio(3);
    if (lane < 16) *(volatile LAS u32*)(L_FLAG + 4 * lane) = 0u;
    asm volatile("" ::"s"(pws), "s"(pwq), "s"(pkc), "s"(pvc), "s"(pwo), "s"(pw1), "s"(pwm));
    u32 due = cyc(), pace = isM ? PACE_M : PACE_A;
    u32 hnext = due + HPOLL0;
    due += KSTART;
    STAMP(0);
    wg_barrier();
    if (isM) wg_barrier();
#define PACED(p) do { u32 now_; while ((int)((now_ = cyc()) - due) < 0) {} due = ((int)(now_ - due) > 2 * (p) ? now_ - 2 * (p) : due) + (p); } while (0)
    {
      const v4i rq = mkrs(pwq, 512 * 4096 * 2);
      const u32 vq = (role * 16 + (lane >> 3)) * 8192 + x * 1024 + (i >> 2) * 128 + (lane & 7) * 16;
#pragma unroll 1
      for (int j = 0; j < 4; j++) {
        PACED(pace);
#pragma unroll
        for (int n = 0; n < 4; n++) dma1(rq, vq, (j * 128 + (n >> 1) * 64 + (n & 1) * 8) * 8192, L_T1 + (j * 4 + n) * 1024);
      }
    }
    const u32 vo = ((b * 8 + x) * 8192 + 6144) * 256u + (lane >> 4) * 256 + 16 * ((lane & 15) ^ (4 * (lane >> 4)));
    const v4i rw4 = mkrs(pws, WS_END);
    const u32 vh = WS_H + role * 2048 + lane * 16;
    {
      u32 ep;
      asm volatile("s_load_dword %0, %1, 0x0\n\ts_waitcnt lgkmcnt(0)" : "=&s"(ep) : "s"(pws) : "memory");
      *(LAS v4u*)(L_HF + lane * 16) = v4u{ep, ep, ep, ep};
      *(LAS v4u*)(L_HF + 1024 + lane * 16) = v4u{ep, ep, ep, ep};
      FENCE();
    }
#define HPOLL() do { if ((int)(due - hnext) >= 0 && flag_get(F_H) == 0) { \
      asm volatile("s_mov_b32 m0, %3\n\ts_nop 0\n\tbuffer_load_dwordx4 %1, %0, %2 offen sc1 lds\n\tbuffer_load_dwordx4 %1, %0, %2 offen offset:1024 sc1 lds" \
                   ::"s"(rw4), "v"(vh), "s"(((due >> HSLOT) & 7) * 8192), "s"(L_HF) : "memory"); \
      hnext = (due | ((1u << HSLOT) - 1)) + 1; } } while (0)
#define CHUNK(j) ((j) * 32768 + ((s + (j) + x + 2 * b) & 7) * 4096)
    {
      const v4i rk = mkrs(pkc, 67108864);
#pragma unroll 1
      for (int j = 0; j < 16; j++) {
        PACED(pace);
        HPOLL();
        dma4(rk, vo, CHUNK(j), L_K + j * 4096);
        if (j == 3) {
          VMWAIT(16); flag_set(F_WQ, 1); STAMP(1);
          if (!isM) pace = PACE_B;
          const v4u ones = {~0u, ~0u, ~0u, ~0u};
          const u32 a0 = WS_O + (x * 32 + i) * 1024 + lane * 16;
          const u32 a1 = lane < 4 ? WS_ML + x * 1024 + lane * 256 + b * 64 + s * 8 : lane >= 32 && role != 3 ? (isO ? WS_X + x * 4096 + oc * 256 : WS_MODR + u * 256) + (lane - 32) * 8 : WS_END;
          asm volatile("buffer_store_dwordx4 %0, %2, %4, 0 offen sc1\n\tbuffer_store_dwordx4 %0, %2, %4, %5 offen sc1\n\t"
                       "buffer_store_dwordx2 %1, %3, %4, 0 offen sc1\n\ts_nop 1"
                       ::"v"(ones), "v"(v2u{~0u, ~0u}), "v"(a0), "v"(a1), "s"(rw4), "s"(WS_Y - WS_O) : "memory");
        }
        if (j == 8) { VMWAIT(20); flag_set(F_WQ, 2); STAMP(23); }
        VMWAIT(WIN - 4);
      }
    }
    {
      const v4i rv = mkrs(pvc, 67108864);
#pragma unroll 1
      for (int j = 0; j < 16; j++) {
        if (j == 12 && isM) stage_wait(1, lane);
        PACED(pace);
        HPOLL();
        dma4(rv, vo, CHUNK(j), L_V + j * 4096);
        if (j == 4) { VMWAIT(20); flag_set(F_K, 1); STAMP(2); }
        VMWAIT(WIN - 4);
      }
    }
    STAMP(3);
    for (u32 spin = 0; flag_get(F_H) == 0 && spin < SPIN_CAP; spin++) { PACED(pace); HPOLL(); VMWAIT(0); }
    flag_set(F_H, 1);
    const bool early = role != 3 && __all(*(volatile LAS u32*)(L_FLAG + 4 * (lane & 3)) >= 3u);
    if (!early) {
      VMWAIT(16); flag_set(F_V, 6); VMWAIT(8); flag_set(F_V, 7); VMWAIT(0); flag_set(F_V, 8); STAMP(4);
      if (role == 3) { if (!isF) return; due += F_DELAY; }
      stage_wait(3, lane);
    }
    const u32 stride = isO ? 1024 : isF ? 3584 : 6144;
    const v4i rl = isO ? mkrs(pwo, 4096 * 512 * 2) : isF ? mkrs(pw1, 512 * 1792 * 2) : mkrs(pwm, 512 * 3072 * 2);
    const u32 col0 = isO ? x * 512 * 1024 + oc * 64 : isF ? u * 64 : 2048 + u * 64;
    const u32 vl = (isO ? (lane >> 3) + 16 * ((lane >> 2) & 1) : lane >> 2) * stride + (lane & 3) * 16 + col0;
    const int half = 4 * ((isO ? oc : u) & 1);
#pragma unroll 1
    for (int g = 0; g < 8; g++) {
      if (g < 4) PACED(PACE_L);
#pragma unroll
      for (int n4 = 0; n4 < 4; n4++) {
        const int n = ((g + half) & 7) * 4 + n4;
        const u32 row = isO ? 128 * ((n >> 1) & 3) + 32 * (n >> 3) + 8 * (n & 1) : 16 * n;
        dma1(rl, vl, row * stride, L_K + n * 1024);
      }
      if (g == 1 && early) { VMWAIT(24); flag_set(F_V, 6); VMWAIT(16); flag_set(F_V, 7); VMWAIT(8); flag_set(F_V, 8); STAMP(4); }
    }
    VMWAIT(0);
    flag_set(F_LATE, 1);
    STAMP(5);
    return;
  }

  const int cw = wave, g = lane >> 4, c = lane & 15, q4 = (lane >> 2) & 3;
  const auto rws = __builtin_amdgcn_make_buffer_rsrc(pws, 0, WS_END, 0x00020000);
  const u32 trow = 128 * cw + 32 * g + q4;
  const u32 kel = 128 * cw + 32 * g + 8 * (c & 3);
  const u32 obase = WS_O + (x * 32 + i) * 1024 + cw * 256 + lane * 4;
  v4u sc_frag = {0, 0, 0, 0};
  u32 tag;
  if (isM) {
    const v4i rm = mkrs(pwm, 512 * 3072 * 2);
    const u32 vm = (lane >> 1) * 6144 + (lane & 1) * 1024 + u * 16;
#define MDMA(n) dma1(rm, vm, (4 * cw + ((u + (n)) & 3)) * 32 * 6144, L_T2 + (4 * cw + ((u + (n)) & 3)) * 1024)
    v4u cr, xr; u32 xe;
    asm volatile("global_load_dwordx4 %0, %3, %6\n\tglobal_load_dwordx4 %1, %4, %7\n\tglobal_load_dword %2, %5, %7"
                 : "=&v"(cr), "=&v"(xr), "=&v"(xe)
                 : "v"(((c >> 2) * 512 + kel) * 2), "v"(cw * 1024 + lane * 16), "v"((g * 512 + u * 8 + (c & 6)) * 2), "s"(pc), "s"(px)
                 : "memory");
    MDMA(0);
    __builtin_amdgcn_s_barrier();
    MDMA(1); MDMA(2); MDMA(3);
    __builtin_amdgcn_s_barrier();
    asm volatile("s_load_dword %3, %4, 0x0\n\ts_waitcnt vmcnt(4) lgkmcnt(0)" : "+v"(cr), "+v"(xr), "+v"(xe), "=&s"(tag) : "s"(pws) : "memory");
    tag += 1u;
#pragma unroll
    for (int e = 0; e < 4; e++) {
      const float lo = bflo(cr[e]), hi = bfhi(cr[e]);
      sc_frag[e] = pk(lo * __builtin_amdgcn_rcpf(1.f + fexp(-lo)), hi * __builtin_amdgcn_rcpf(1.f + fexp(-hi)));
    }
    v2f w[4] = {unpk(xr.x), unpk(xr.y), unpk(xr.z), unpk(xr.w)};
    const v2f t = (w[0] + w[1]) + (w[2] + w[3]);
    const float mean = wave_sum(t[0] + t[1]) * (1.f / 512.f);
    v2f sq = {0.f, 0.f};
#pragma unroll
    for (int j = 0; j < 4; j++) { const v2f d = w[j] - mean; sq += d * d; }
    const float den = rb(__builtin_amdgcn_sqrtf(rb(rb(wave_sum(sq[0] + sq[1]) * (1.f / 512.f)) + 1e-5f)));
    *(LAS v2f*)(L_LN + 8 * cw) = v2f{rb(mean), __builtin_amdgcn_rcpf(den)};
    PIN(sc_frag);
    VMWAIT(0);
    STAMP0(6);
    v4u wf[4];
#pragma unroll
    for (int k = 0; k < 4; k++) wf[k] = tr2(L_T2 + trow * 32 + 8 * (lane & 3) + 256 * k, L_T2 + trow * 32 + 8 * (lane & 3) + 256 * k + 128);
    *(LAS float*)(L_RM + lane * 16 + cw * 4) = dot_sets(sc_frag, wf);
    flag_set(cw, 1);
    if (cw == 0) {
      stage_wait(1, lane);
      const v4f p = *(LAS v4f*)(L_RM + lane * 16);
      const v2f ln = *(LAS v2f*)(L_LN + 8 * g);
      const float md = rb((p[0] + p[1]) + (p[2] + p[3]));
      const float h = rb(rb(rb(((c & 1) ? bfhi(xe) : bflo(xe)) - ln[0]) * ln[1]) * rb(1.f + dppf<ROR8>(md))) + md;
      const u32 hp = pk(h, dppf<QX1>(h));
      if ((lane & 9) == 0) *(LAS u32*)(L_RM + g * 16 + c * 2) = hp;
      FENCE();
      const v2u hq = *(LAS v2u*)(L_RM + (lane & 7) * 8);
      __builtin_amdgcn_raw_buffer_store_b128(v4u{hq.x, tag, hq.y, tag}, rws, WS_H + (lane >> 3) * 8192 + ((u >> 1) & 3) * 2048 + (x * 2 + (u & 1)) * 128 + (lane & 7) * 16, 0, SC1);
      STAMP(7);
    }
  } else {
    __builtin_amdgcn_s_barrier();
    asm volatile("s_load_dword %0, %1, 0x0\n\ts_waitcnt lgkmcnt(0)" : "=&s"(tag) : "s"(pws) : "memory");
    tag += 1u;
    if (i == 1) {
      unsigned long long pc_;
      asm volatile("s_getpc_b64 %0" : "=s"(pc_));
      u32 d_;
      asm volatile("global_load_dword %0, %1, %2\n\ts_waitcnt vmcnt(0)" : "=&v"(d_) : "v"(min((cw * 64 + lane) * 64, CODE_TAIL)), "s"(pc_) : "memory");
    }
    if (isF) {
      const v4u xr = *(const GAS v4u*)(px + cw * 512 + lane * 8);
      const v2f t = (unpk(xr.x) + unpk(xr.y)) + (unpk(xr.z) + unpk(xr.w));
      *(LAS float*)(L_LN + 4 * cw) = wave_sum(t[0] + t[1]) * (1.f / 512.f);
    }
  }
  u32 qdue;
  {
    v4u wf[4];
    flag_wait(F_WQ, 1);
#pragma unroll
    for (int k = 0; k < 4; k++) {
      const u32 a = L_T1 + (32 * g + q4) * 128 + cw * 32 + 8 * (lane & 3) + 1024 * k;
      wf[k] = tr2(a, a + 512);
    }
    PIN4(wf);
    u32 qo = WS_Q + (g * 8 + x) * 16384 + b * 4096 + role * 1024 + ((i >> 2) & 1) * 512 + cw * 128 + c * 8;
    u32 ha = L_HF + (4 * g + (c & 3)) * 128 + (c >> 2) * 32;
    PIN(qo); PIN(ha);
    flag_wait(F_WQ, 2);
    STAMP0(21);
    v4u r0, r1;
    for (;;) {
      FENCE();
      const u32 giveup = *(volatile LAS u32*)(L_FLAG + 4 * F_V);
      r0 = lds4(ha); r1 = lds4(ha + 16);
      if (__all(((r0.y ^ tag) | (r0.w ^ tag) | (r1.y ^ tag) | (r1.w ^ tag)) == 0u || giveup != 0u)) break;
    }
    flag_set(cw, 2);
    STAMP0(8);
    __builtin_amdgcn_raw_buffer_store_b64(v2u{__float_as_uint(dot_sets(v4u{r0.x, r0.z, r1.x, r1.z}, wf)), tag}, rws, qo, 0, SC1);
    qdue = cyc() + Q_DELAY;
    if (cw == 0) { stage_wait(2, lane); flag_set(F_H, 1); }
    STAMP0(9);
  }
  v4u kf[4][4];
  flag_wait(F_K, 1);
  {
    const u32 ka = L_K + cw * 16384 + 256 * c + 64 * (c & 3) + 16 * g;
#pragma unroll
    for (int j = 0; j < 4; j++)
#pragma unroll
      for (int t = 0; t < 4; t++) kf[j][t] = lds4((ka ^ (64 * t)) + 4096 * j);
  }
  flag_set(cw, 3);
  v4u pf[8];
  float oscale;
  {
    while ((int)(cyc() - qdue) < 0) {}
    STAMP0(22);
    {
      v4u r[4];
      for (u32 spin = 0; spin < SPIN_CAP; spin++) {
        FENCE();
        u32 bad = 0;
#pragma unroll
        for (int k = 0; k < 4; k++) r[k] = __builtin_amdgcn_raw_buffer_load_b128(rws, WS_Q + (b * 8 + x) * 16384 + cw * 4096 + lane * 16 + k * 1024, 0, SC1);
#pragma unroll
        for (int k = 0; k < 4; k++) bad |= (r[k].y ^ tag) | (r[k].w ^ tag);
        if (__all(bad == 0u)) break;
      }
      *(LAS u32*)(L_QL + cw * QL_S + lane * 4) = pk((__uint_as_float(r[0].x) + __uint_as_float(r[1].x)) + (__uint_as_float(r[2].x) + __uint_as_float(r[3].x)),
                                                    (__uint_as_float(r[0].z) + __uint_as_float(r[1].z)) + (__uint_as_float(r[2].z) + __uint_as_float(r[3].z)));
    }
    flag_set(cw, 4);
    stage_wait(4, lane);
    if (i == 1 && cw >= 2)
      __builtin_amdgcn_raw_buffer_store_b32(tag, rws, WS_GC + ((cw - 2) * 64 + lane) * 128 + x * 4, 0, SC1);
    STAMP0(10);
    const u32 qa = L_QL + (c >> 2) * QL_S + 16 * g;
    const v4u qf[4] = {lds4(qa), lds4(qa + 64), lds4(qa + 128), lds4(qa + 192)};
    v4f a[4];
#pragma unroll
    for (int t = 0; t < 4; t++)
#pragma unroll
      for (int j = 0; j < 4; j++) a[j] = t ? mfma(kf[j][t], qf[t], a[j]) : mfma0(kf[j][0], qf[0]);
    const v4f a01 = (c & 1) ? a[1] : a[0], a23 = (c & 1) ? a[3] : a[2], sa = (c & 2) ? a23 : a01;
    const float SC = 0.08838834764831845f;
    const v2f s0 = rb2(rb2(v2f{sa[0], sa[1]}) * SC), s1 = rb2(rb2(v2f{sa[2], sa[3]}) * SC);
    float m = fmx(fmx3(s0[0], s0[1], s1[0]), s1[1]);
    m = fmaxf(m, dppf<QX1>(m)); m = fmaxf(m, dppf<QX2>(m)); m = xg_max(m);
    *(LAS float*)(L_MX + (c >> 2) * 16 + cw * 4) = m;
    flag_set(cw, 5);
    stage_wait(5, lane);
    const v4f mx = *(LAS v4f*)(L_MX + (c >> 2) * 16), mh = *(LAS v4f*)(L_MX + g * 16);
    m = fmx(fmx3(mx[0], mx[1], mx[2]), mx[3]);
    const float p0 = fexp(s0[0] - m), p1 = fexp(s0[1] - m), p2 = fexp(s1[0] - m), p3 = fexp(s1[1] - m);
    const u32 pa = L_PL + (c >> 2) * 64 + g * 16;
    *(LAS v2u*)(pa + (2 * cw + ((c >> 1) & 1)) * 256 + (c & 1) * 8) = v2u{pk(p0, p1), pk(p2, p3)};
    *(LAS float*)(L_SM + (c >> 2) * 16 + cw * 4) = xg_sum(quad_sum((p0 + p1) + (p2 + p3)));
    flag_set(cw, 6);
    stage_wait(6, lane);
    const v4f sm = *(LAS v4f*)(L_SM + g * 16);
    const float ml2 = fmx(fmx3(mh[0], mh[1], mh[2]), mh[3]) * 1.4426950408889634f, me = __builtin_ceilf(ml2);
    oscale = __builtin_amdgcn_exp2f(ml2 - me);
    if (cw == 0 && c == 0)
      __builtin_amdgcn_raw_buffer_store_b64(v2u{nosent(__float_as_uint(me)), nosent(__float_as_uint(((sm[0] + sm[1]) + (sm[2] + sm[3])) * oscale))}, rws,
                                            WS_ML + x * 1024 + g * 256 + b * 64 + s * 8, 0, SC1);
#pragma unroll
    for (int k = 0; k < 8; k++) pf[k] = lds4(pa + k * 256);
    PIN8(pf);
    PIN(oscale);
    STAMP0(12);
  }
  {
    const u32 va = L_V + 256 * (4 * g + q4) + 64 * (cw ^ q4) + 8 * (lane & 3);
    v4f o0, o1;
    flag_wait(F_V, 6);
    {
      v4u vf[6][2];
#pragma unroll
      for (int k = 0; k < 6; k++) { vf[k][0] = tr2(va + 8192 * k, va + 8192 * k + 4096); vf[k][1] = tr2(va + 8192 * k + 32, va + 8192 * k + 4096 + 32); }
      SCHED();
      o0 = mfma0(pf[0], vf[0][0]); o1 = mfma0(pf[0], vf[0][1]);
#pragma unroll
      for (int k = 1; k < 6; k++) { o0 = mfma(pf[k], vf[k][0], o0); o1 = mfma(pf[k], vf[k][1], o1); }
    }
#pragma unroll
    for (int k = 6; k < 8; k++) {
      flag_wait_hot(F_V, k + 1);
      o0 = mfma(pf[k], tr2(va + 8192 * k, va + 8192 * k + 4096), o0);
      o1 = mfma(pf[k], tr2(va + 8192 * k + 32, va + 8192 * k + 4096 + 32), o1);
    }
    __builtin_amdgcn_raw_buffer_store_b32(nosent(pk(o0[0] * oscale, o1[0] * oscale)), rws, obase, 0, SC1);
  }
  STAMP0(13);
  if (isM | isO | isF) {
    const u32 hm = (c & 1) ? 0xffff0000u : 0x0000ffffu, mk0 = (c & 2) ? 0u : hm, mk1 = (c & 2) ? hm : 0u;
    const u32 ta = L_K + trow * 64 + 8 * (lane & 3), rw = L_RED + ((cw * 4 + g) * 32 + c) * 4;
    v4u wa[4], wb[4];
    if (!isF) {
      v4u af = sc_frag;
      v4u r[8];
      v2u w[8];
      float linv = 0.f;
      const u32 og = WS_O + (x * 32 + (c >> 2) * 8) * 1024 + cw * 256 + g * 64 + (c & 3) * 16;
      if (isO) {
        v4u ml = __builtin_amdgcn_raw_buffer_load_b128(rws, WS_ML + x * 1024 + lane * 16, 0, SC1);
        gather_issue<1024>(rws, og, r);
        for (u32 spin = 0; __any(umax4(ml) == 0xffffffffu) && spin < SPIN_CAP; spin++) { FENCE(); ml = __builtin_amdgcn_raw_buffer_load_b128(rws, WS_ML + x * 1024 + lane * 16, 0, SC1); }
        float M = fmx(__uint_as_float(ml.x), __uint_as_float(ml.z));
        M = fmaxf(M, dppf<QX1>(M)); M = fmaxf(M, dppf<QX2>(M));
        const float w0 = __builtin_amdgcn_exp2f(__uint_as_float(ml.x) - M), w1 = __builtin_amdgcn_exp2f(__uint_as_float(ml.z) - M);
        linv = __builtin_amdgcn_rcpf(quad_sum(w0 * __uint_as_float(ml.y) + w1 * __uint_as_float(ml.w)));
        const u32 p0 = pk(w0, w0), p1 = pk(w1, w1);
        w[0] = v2u{dppu<0x00>(p0) & mk0, dppu<0x00>(p0) & mk1}; w[1] = v2u{dppu<0x00>(p1) & mk0, dppu<0x00>(p1) & mk1};
        w[2] = v2u{dppu<0x55>(p0) & mk0, dppu<0x55>(p0) & mk1}; w[3] = v2u{dppu<0x55>(p1) & mk0, dppu<0x55>(p1) & mk1};
        w[4] = v2u{dppu<0xAA>(p0) & mk0, dppu<0xAA>(p0) & mk1}; w[5] = v2u{dppu<0xAA>(p1) & mk0, dppu<0xAA>(p1) & mk1};
        w[6] = v2u{dppu<0xFF>(p0) & mk0, dppu<0xFF>(p0) & mk1}; w[7] = v2u{dppu<0xFF>(p1) & mk0, dppu<0xFF>(p1) & mk1};
        STAMP0(11);
      }
      flag_wait_hot(F_LATE, 1);
#pragma unroll
      for (int k = 0; k < 4; k++) { wa[k] = tr2(ta + 512 * k, ta + 512 * k + 256); wb[k] = tr2(ta + 512 * k + 32, ta + 512 * k + 32 + 256); }
      PIN4(wa); PIN4(wb);
      STAMP0(14);
      if (isO) {
        v4f a0, a1;
        gather_sum<1024>(rws, og, r, w, a0, a1);
        STAMP0(16);
        a0 *= linv; a1 *= linv;
        af = v4u{pk(a0[0], a0[1]), pk(a0[2], a0[3]), pk(a1[0], a1[1]), pk(a1[2], a1[3])};
      }
      *(LAS float*)rw = dot_sets(af, wa);
      *(LAS float*)(rw + 64) = dot_sets(af, wb);
      wg_barrier();
      if (cw == 0) {
        const u32 ra = L_RED + lane * 8;
        const v2f t = (*(LAS v2f*)ra + *(LAS v2f*)(ra + 512)) + (*(LAS v2f*)(ra + 1024) + *(LAS v2f*)(ra + 1536));
        __builtin_amdgcn_raw_buffer_store_b32(nosent(pk2(t)), rws, (isO ? WS_X + x * 4096 + oc * 256 : WS_MODR + u * 256) + lane * 4, 0, SC1);
        STAMP(17);
      } else if (cw == 1 && isM) {
        const int ub = u >> 4, ct = u & 15, el = 8 * (c & 3) + 4 * (g & 1);
        const v2u xv = *(const GAS v2u*)(px + ub * 512 + ct * 32 + el), b2v = *(const GAS v2u*)(pb2 + ct * 32 + el);
        const u32 one = 0x3F803F80u & hm;
        const v4u sl = {(c >> 1) == 0 ? one : 0u, (c >> 1) == 1 ? one : 0u, (c >> 1) == 2 ? one : 0u, (c >> 1) == 3 ? one : 0u};
        marks_wait(rws, WS_GC + u * 128 + (lane & 7) * 4, tag);
        v2u gav, gbv; v4u xp = {0, 0, 0, 0};
        for (u32 spin = 0; spin < SPIN_CAP; spin++) {
          FENCE();
          gav = __builtin_amdgcn_raw_buffer_load_b64(rws, WS_MODR + ct * 256 + ub * 64 + el * 2, 0, SC1);
          gbv = __builtin_amdgcn_raw_buffer_load_b64(rws, WS_MODR + 12288 + ct * 256 + ub * 64 + el * 2, 0, SC1);
          if (lane < 32) xp = __builtin_amdgcn_raw_buffer_load_b128(rws, WS_X + (lane >> 2) * 4096 + ct * 256 + ub * 64 + (lane & 3) * 16, 0, SC1);
          if (__all(max(max(max(gav.x, gav.y), max(gbv.x, gbv.y)), umax4(xp)) != 0xffffffffu)) break;
          __builtin_amdgcn_s_sleep(1);
        }
        v4f at = mfma0(sl, xp);
#pragma unroll
        for (int r = 0; r < 4; r++) { at[r] += dppf<ROR4>(at[r]); at[r] += dppf<ROR8>(at[r]); }
        v2f x10 = rb2(unpk(xv.x) + rb2(unpk(gav.x) * rb2(v2f{at[0], at[1]}))), x11 = rb2(unpk(xv.y) + rb2(unpk(gav.y) * rb2(v2f{at[2], at[3]})));
        v2f g0 = unpk(gbv.x), g1 = unpk(gbv.y);
        x10 += g0 * unpk(b2v.x); x11 += g1 * unpk(b2v.y);
        PIN(x10); PIN(x11); PIN(g0); PIN(g1);
        STAMP(18);
        v4u y[4];
        v4f ys;
        y[3] = v4u{0, 0, 0, 0};
        for (u32 spin = 0;; spin++) {
          FENCE();
#pragma unroll
          for (int k = 0; k < 3; k++) y[k] = __builtin_amdgcn_raw_buffer_load_b128(rws, WS_Y + u * 4096 + k * 1024 + lane * 16, 0, SC1);
          if (lane < 32) y[3] = __builtin_amdgcn_raw_buffer_load_b128(rws, WS_Y + u * 4096 + 3072 + lane * 16, 0, SC1);
          ys = mfma0(sl, y[0]); ys = mfma(sl, y[1], ys); ys = mfma(sl, y[2], ys); ys = mfma(sl, y[3], ys);
          if (__builtin_expect(!any_nan(ys[0], ys[0]), 1)) break;
          if (__all(max(max(umax4(y[0]), umax4(y[1])), max(umax4(y[2]), umax4(y[3]))) != 0xffffffffu) || spin >= SPIN_CAP) break;
        }
        STAMP(19);
#pragma unroll
        for (int r = 0; r < 4; r++) { ys[r] += dppf<ROR4>(ys[r]); ys[r] += dppf<ROR8>(ys[r]); }
        if (lane < 32 && c < 4) *(GAS v2u*)(pout + ub * 512 + ct * 32 + el) = v2u{pk2(x10 + g0 * v2f{ys[0], ys[1]}), pk2(x11 + g1 * v2f{ys[2], ys[3]})};
        if (u == 0 && lane == 0) __builtin_amdgcn_raw_buffer_store_b32(tag, rws, WS_EPOCH, 0, SC1);
        STAMP(20);
      }
    } else {
      v4u w2f[8];
      {
        v4u w2r[8];
        const auto r2 = __builtin_amdgcn_make_buffer_rsrc((void*)pw2, 0, 1792 * 512 * 2, 0x00020000);
        const u32 o = (u * 32 + 8 * g) * 1024 + cw * 256 + c * 16;
#pragma unroll
        for (int r = 0; r < 8; r++) w2r[r] = __builtin_amdgcn_raw_buffer_load_b128(r2, o + (r & 3) * 1024, (r >> 2) * 4096, NT);
#pragma unroll
        for (int j = 0; j < 8; j++) {
#pragma unroll
          for (int e = 0; e < 4; e++) w2f[j][e] = __builtin_amdgcn_perm(w2r[2 * e + 1][j >> 1], w2r[2 * e][j >> 1], (j & 1) ? 0x07060302u : 0x05040100u);
          PIN(w2f[j]);
        }
      }
      const u32 b1p = *(const GAS u32*)(pb1 + u * 32 + 8 * g + 2 * (c & 3));
      const v4u xq = *(const GAS v4u*)(px + (c >> 2) * 512 + kel);
      flag_wait(F_LATE, 1);
#pragma unroll
      for (int k = 0; k < 4; k++) { wa[k] = tr2(ta + 512 * k, ta + 512 * k + 256); wb[k] = tr2(ta + 512 * k + 32, ta + 512 * k + 32 + 256); }
      PIN4(wa); PIN4(wb);
      v4u gq, sf, cq;
      marks_wait(rws, WS_GC + (64 + u) * 128 + (lane & 7) * 4, tag);
      for (u32 spin = 0; spin < SPIN_CAP; spin++) {
        FENCE();
        gq = __builtin_amdgcn_raw_buffer_load_b128(rws, WS_MODR + cw * 1024 + lane * 16, 0, SC1);
        sf = __builtin_amdgcn_raw_buffer_load_b128(rws, WS_MODR + cw * 1024 + lane * 16, 4096, SC1);
        cq = __builtin_amdgcn_raw_buffer_load_b128(rws, WS_MODR + cw * 1024 + lane * 16, 8192, SC1);
        if (__all(max(max(umax4(gq), umax4(sf)), umax4(cq)) != 0xffffffffu)) break;
      }
      STAMP0(15);
      const float piv = *(LAS float*)(L_LN + (c >> 2) * 4);
      v4u gf;
      v2f ga[4], xz[4], gg[4], xg[4];
#pragma unroll
      for (int e = 0; e < 4; e++) {
        gf[e] = pk2(unpk(cq[e]) + 1.f);
        ga[e] = unpk(gq[e]); xz[e] = unpk(xq[e]) - piv; gg[e] = ga[e] * unpk(gf[e]); xg[e] = xz[e] * unpk(gf[e]);
        PIN(ga[e]); PIN(xz[e]); PIN(gg[e]); PIN(xg[e]);
      }
      *(LAS float*)(rw - L_RED + L_TS) = dot_sets(gf, wa);
      *(LAS float*)(rw - L_RED + L_TS + 64) = dot_sets(gf, wb);
      *(LAS float*)(rw - L_RED + L_TS + 2048) = dot_sets(sf, wa);
      *(LAS float*)(rw - L_RED + L_TS + 2048 + 64) = dot_sets(sf, wb);
      wg_barrier();
      const u32 ra = ((c >> 2) * 32 + 8 * g + 2 * (c & 3)) * 4;
      v2f T = (*(LAS v2f*)(L_TS + ra) + *(LAS v2f*)(L_TS + ra + 512)) + (*(LAS v2f*)(L_TS + ra + 1024) + *(LAS v2f*)(L_TS + ra + 1536));
      v2f S = (*(LAS v2f*)(L_TS + ra + 2048) + *(LAS v2f*)(L_TS + ra + 2560)) + (*(LAS v2f*)(L_TS + ra + 3072) + *(LAS v2f*)(L_TS + ra + 3584)) + unpk(b1p);
      PIN(T); PIN(S);
      v4f a0, a1;
      v4u r[8];
      const v2u id = {0x3F803F80u & mk0, 0x3F803F80u & mk1}, w[8] = {id, id, id, id, id, id, id, id};
      gather_issue<4096>(rws, WS_X + cw * 1024 + lane * 16, r);
      gather_sum<4096>(rws, WS_X + cw * 1024 + lane * 16, r, w, a0, a1);
      STAMP0(16);
      const v2f at[4] = {v2f{a0[0], a0[1]}, v2f{a0[2], a0[3]}, v2f{a1[0], a1[1]}, v2f{a1[2], a1[3]}};
      const v4u af = {pk2(at[0] * gg[0] + xg[0]), pk2(at[1] * gg[1] + xg[1]), pk2(at[2] * gg[2] + xg[2]), pk2(at[3] * gg[3] + xg[3])};
      const float da = dot_sets(af, wa), db = dot_sets(af, wb);
      v2f s1 = {0.f, 0.f}, s2 = {0.f, 0.f};
#pragma unroll
      for (int e = 0; e < 4; e++) { const v2f z = at[e] * ga[e] + xz[e]; s1 += z; s2 += z * z; }
      *(LAS v2f*)(L_ST + (c >> 2) * 32 + cw * 8) = v2f{xg_sum(quad_sum(s1[0] + s1[1])), xg_sum(quad_sum(s2[0] + s2[1]))};
      *(LAS float*)rw = da;
      *(LAS float*)(rw + 64) = db;
      wg_barrier();
      const v4f st0 = *(LAS v4f*)(L_ST + (c >> 2) * 32), st1 = *(LAS v4f*)(L_ST + (c >> 2) * 32 + 16);
      const v2f P = (*(LAS v2f*)(L_RED + ra) + *(LAS v2f*)(L_RED + ra + 512)) + (*(LAS v2f*)(L_RED + ra + 1024) + *(LAS v2f*)(L_RED + ra + 1536));
      const v2f st = ((v2f{st0[0], st0[1]} + v2f{st0[2], st0[3]}) + (v2f{st1[0], st1[1]} + v2f{st1[2], st1[3]})) * (1.f / 512.f);
      const float rs = __builtin_amdgcn_rsqf(fmaxf(st[1] - st[0] * st[0], 0.f) + 1e-5f);
      const v2f pre = (P - T * st[0]) * rs + S;
      const float h0 = pre[0] > 0.f ? pre[0] : 0.f, h1 = pre[1] > 0.f ? pre[1] : 0.f;
      const u32 hp = pk(h0 * h0, h1 * h1);
      const v4u hf = {dppu<0x00>(hp), dppu<0x55>(hp), dppu<0xAA>(hp), dppu<0xFF>(hp)};
      float y[8];
#pragma unroll
      for (int j = 0; j < 8; j++) y[j] = mfma0(hf, w2f[j])[0];
      __builtin_amdgcn_raw_buffer_store_b128(v4u{nosent(pk(y[0], y[1])), nosent(pk(y[2], y[3])), nosent(pk(y[4], y[5])), nosent(pk(y[6], y[7]))}, rws,
                                             WS_Y + (g * 16 + 4 * cw + (c >> 2)) * 4096 + u * 64 + (c & 3) * 16, 0, SC1);
      STAMP0(17);
    }
  }
}

extern "C" void kernel_launch(void* const* d_in, const int* in_sizes, int n_in, void* d_out, int out_size, void* d_ws, size_t ws_size, hipStream_t stream) {
  fused_block<<<256, 320, 0, stream>>>((unsigned char*)d_ws, (const u16*)d_in[3], (const u16*)d_in[1], (const u16*)d_in[2], (const u16*)d_in[0], (const u16*)d_in[10],
                                       (const u16*)d_in[4], (const u16*)d_in[6], (const u16*)d_in[11], (const u16*)d_in[7], (const u16*)d_in[8], (const u16*)d_in[9],
                                       (u16*)d_out);
}
```

```cpp
#include <hip/hip_runtime.h>
#include <hip/hip_bf16.h>
#pragma clang diagnostic ignored "-Wint-to-pointer-cast"

typedef unsigned u32;
typedef unsigned short u16;
typedef u32 v4u __attribute__((ext_vector_type(4)));
typedef u32 v2u __attribute__((ext_vector_type(2)));
typedef int v4i __attribute__((ext_vector_type(4)));
typedef float v4f __attribute__((ext_vector_type(4)));
typedef float v2f __attribute__((ext_vector_type(2)));
typedef short v4s __attribute__((ext_vector_type(4)));
typedef __bf16 v8bf __attribute__((ext_vector_type(8)));
typedef __bf16 v2bf __attribute__((ext_vector_type(2)));
#define LAS __attribute__((address_space(3)))
#define GAS __attribute__((address_space(1)))
#define DEV __device__ __forceinline__
#define FENCE() asm volatile("" ::: "memory")
#define SCHED() __builtin_amdgcn_sched_barrier(0)
#define PIN(v) asm volatile("" : "+v"(v))
#define PIN4(a) asm volatile("" : "+v"(a[0]), "+v"(a[1]), "+v"(a[2]), "+v"(a[3]))
#define PIN8(a) asm volatile("" : "+v"(a[0]), "+v"(a[1]), "+v"(a[2]), "+v"(a[3]), "+v"(a[4]), "+v"(a[5]), "+v"(a[6]), "+v"(a[7]))

#define PACE_A 195
#define PACE_B 248
#define PACE_M 193
#define KSTART 200
#define PACE_L 290
#define F_DELAY 1150
#define HSLOT 9
#define HPOLL0 2700
#define Q_DELAY 1500
#define WIN 32

constexpr u32 WS_EPOCH = 0, WS_MODR = 49152, WS_X = 65536, WS_ML = 98304, WS_GC = 106496, WS_O = 131072, WS_Y = 393216, WS_H = 655360, WS_Q = 720896,
              WS_END = WS_Q + 524288;
constexpr u32 WS_STAMP = 1u << 21;
constexpr u32 L_K = 0, L_V = 65536, L_T2 = 114688, L_T1 = 131072, L_RM = 147456, L_HF = L_RM + 1024, L_RED = L_HF + 2048, L_TS = L_RED + 2048, L_PL = L_TS + 4096,
              L_QL = L_PL + 2048, QL_S = 288, L_MX = L_QL + 4 * QL_S, L_SM = L_MX + 64, L_LN = L_SM + 64, L_ST = L_LN + 64, L_FLAG = L_ST + 128, L_END = L_FLAG + 64;
static_assert(L_END + 128 <= 163840, "lds");
enum { F_K = 4, F_V, F_LATE, F_WQ, F_H };
constexpr int CODE_TAIL = 11000;
constexpr u32 SPIN_CAP = 1u << 16;
constexpr int SC1 = 16, NT = 2;

DEV u32 rtv() { unsigned long long t; asm volatile("s_memrealtime %0\n\ts_waitcnt lgkmcnt(0)" : "=s"(t)::"memory"); return (u32)t; }
DEV u32 cyc() { return rtv() << 4; }
#define LEAK 0
#define STAMP(k) do { if ((k) == 0 || (k) == (LEAK & 63)) { SCHED(); u32 t_ = rtv(); *(volatile LAS u32*)(L_STAMP + 4 * (k)) = t_; if ((k) == 0) { *(volatile LAS u32*)(L_STAMP + 4 * (LEAK & 31)) = t_; if (LEAK == 100) __hip_atomic_store((GAS u32*)(pws + WS_STAMP) + wg, t_, __ATOMIC_RELAXED, __HIP_MEMORY_SCOPE_AGENT); } SCHED(); } } while (0)
#define STAMP0(k) do { if (cw == 0) STAMP(k); } while (0)
constexpr u32 L_STAMP = L_END;

DEV float bflo(u32 v) { return __uint_as_float(v << 16); }
DEV float bfhi(u32 v) { return __uint_as_float(v & 0xffff0000u); }
DEV v2f unpk(u32 v) { return v2f{bflo(v), bfhi(v)}; }
DEV u32 pk2(v2f p) { return __builtin_bit_cast(u32, __builtin_convertvector(p, v2bf)); }
DEV u32 pk(float lo, float hi) { return pk2(v2f{lo, hi}); }
DEV float rb(float f) { return bflo(pk(f, f)); }
DEV v2f rb2(v2f p) { return unpk(pk2(p)); }
DEV float fmx(float a, float b) { float r; asm("v_max_f32 %0, %1, %2" : "=v"(r) : "v"(a), "v"(b)); return r; }
DEV float fmx3(float a, float b, float c) { float r; asm("v_max3_f32 %0, %1, %2, %3" : "=v"(r) : "v"(a), "v"(b), "v"(c)); return r; }
DEV float fexp(float a) { return __builtin_amdgcn_exp2f(a * 1.4426950408889634f); }
DEV u32 nosent(u32 v) { return v < 0xfffffffeu ? v : 0xfffffffeu; }
DEV v4f mfma(v4u a, v4u b, v4f c) { return __builtin_amdgcn_mfma_f32_16x16x32_bf16(__builtin_bit_cast(v8bf, a), __builtin_bit_cast(v8bf, b), c, 0, 0, 0); }
DEV v4f mfma0(v4u a, v4u b) { return mfma(a, b, v4f{0.f, 0.f, 0.f, 0.f}); }
DEV v2u trr(u32 addr) { return __builtin_bit_cast(v2u, __builtin_amdgcn_ds_read_tr16_b64_v4i16((LAS v4s*)addr)); }
DEV v4u tr2(u32 a0, u32 a1) { v2u a = trr(a0), b = trr(a1); return v4u{a.x, a.y, b.x, b.y}; }
DEV v4u lds4(u32 addr) { return *(LAS v4u*)addr; }
template <int C> DEV float dppf(float x) { return __builtin_bit_cast(float, __builtin_amdgcn_mov_dpp(__builtin_bit_cast(int, x), C, 0xf, 0xf, true)); }
template <int C> DEV u32 dppu(u32 x) { return (u32)__builtin_amdgcn_mov_dpp((int)x, C, 0xf, 0xf, true); }
template <int C> DEV v4u dpp4(v4u x) { return v4u{dppu<C>(x.x), dppu<C>(x.y), dppu<C>(x.z), dppu<C>(x.w)}; }
constexpr int QX1 = 0xB1, QX2 = 0x4E, ROR4 = 0x124, ROR8 = 0x128;
DEV float xg_sum(float x) {
  auto s = __builtin_amdgcn_permlane16_swap(__float_as_uint(x), __float_as_uint(x), false, false);
  x = __uint_as_float(s[0]) + __uint_as_float(s[1]);
  auto t = __builtin_amdgcn_permlane32_swap(__float_as_uint(x), __float_as_uint(x), false, false);
  return __uint_as_float(t[0]) + __uint_as_float(t[1]);
}
DEV float xg_max(float x) {
  auto s = __builtin_amdgcn_permlane16_swap(__float_as_uint(x), __float_as_uint(x), false, false);
  x = fmaxf(__uint_as_float(s[0]), __uint_as_float(s[1]));
  auto t = __builtin_amdgcn_permlane32_swap(__float_as_uint(x), __float_as_uint(x), false, false);
  return fmaxf(__uint_as_float(t[0]), __uint_as_float(t[1]));
}
DEV float quad_sum(float x) { x += dppf<QX1>(x); return x + dppf<QX2>(x); }
DEV float wave_sum(float x) { x = quad_sum(x); x += dppf<ROR4>(x); x += dppf<ROR8>(x); return xg_sum(x); }
DEV u32 umax4(v4u v) { return max(max(v.x, v.y), max(v.z, v.w)); }
DEV bool any_nan(float a, float b) { return __any(__builtin_isunordered(a, b)); }

DEV v4i mkrs(const void* p, u32 bytes) { v4i r; r.x = (int)(size_t)p; r.y = (int)((size_t)p >> 32) & 0xffff; r.z = (int)bytes; r.w = 0x00020000; return r; }
DEV void dma1(v4i rs, u32 voff, u32 soff, u32 lds) {
  asm volatile("s_mov_b32 m0, %3\n\ts_nop 0\n\tbuffer_load_dwordx4 %1, %0, %2 offen nt lds" ::"s"(rs), "v"(voff), "s"(soff), "s"(lds) : "memory");
}
DEV void dma4(v4i rs, u32 voff, u32 soff, u32 lds) {
  asm volatile("s_mov_b32 m0, %3\n\ts_nop 0\n\tbuffer_load_dwordx4 %1, %0, %2 offen nt lds\n\tbuffer_load_dwordx4 %1, %0, %2 offen offset:1024 nt lds\n\t"
               "buffer_load_dwordx4 %1, %0, %2 offen offset:2048 nt lds\n\tbuffer_load_dwordx4 %1, %0, %2 offen offset:3072 nt lds" ::"s"(rs), "v"(voff), "s"(soff), "s"(lds) : "memory");
}
#define VMWAIT(n) asm volatile("s_waitcnt vmcnt(%0)" ::"i"(n) : "memory")
DEV u32 flag_get(int f) { return __builtin_amdgcn_readfirstlane(*(volatile LAS u32*)(L_FLAG + 4 * f)); }
DEV void flag_set(int f, u32 v) { FENCE(); *(volatile LAS u32*)(L_FLAG + 4 * f) = v; FENCE(); }
DEV void flag_wait(int f, u32 v) { while (flag_get(f) < v) __builtin_amdgcn_s_sleep(1); FENCE(); }
DEV void flag_wait_hot(int f, u32 v) { while (flag_get(f) < v) {} FENCE(); }
DEV void stage_wait(u32 v, int lane) { while (!__all(*(volatile LAS u32*)(L_FLAG + 4 * (lane & 3)) >= v)) {} FENCE(); }
DEV void wg_barrier() { asm volatile("s_waitcnt lgkmcnt(0)\n\ts_barrier" ::: "memory"); }

DEV float dot_sets(v4u a, const v4u (&w)[4]) {
  const v4f d0 = mfma0(a, w[0]), d1 = mfma0(a, w[1]), d2 = mfma0(a, w[2]), d3 = mfma0(a, w[3]);
  return (d0[0] + d1[1]) + (d2[2] + d3[3]);
}
DEV v4f mfma4(v2u a, u32 b0, u32 b1, v4f c) {
  return __builtin_amdgcn_mfma_f32_4x4x4bf16_1k(__builtin_bit_cast(v4s, a), __builtin_bit_cast(v4s, v2u{b0, b1}), c, 0, 0, 0);
}
template <int STRIDE>
DEV void gather_issue(__amdgpu_buffer_rsrc_t rws, u32 voff, v4u (&r)[8]) {
#pragma unroll
  for (int k = 0; k < 8; k++) r[k] = __builtin_amdgcn_raw_buffer_load_b128(rws, voff + (STRIDE < 4096 ? (k & 3) * STRIDE : 0), STRIDE < 4096 ? (k >> 2) * 4 * STRIDE : k * STRIDE, SC1);
}
template <int STRIDE>
DEV void gather_sum(__amdgpu_buffer_rsrc_t rws, u32 voff, v4u (&r)[8], const v2u (&w)[8], v4f& a0, v4f& a1) {
  for (u32 spin = 0;; spin++) {
    a0 = v4f{0.f, 0.f, 0.f, 0.f}; a1 = a0;
#pragma unroll
    for (int k = 0; k < 8; k++) { a0 = mfma4(w[k], r[k].x, r[k].y, a0); a1 = mfma4(w[k], r[k].z, r[k].w, a1); }
    if (__builtin_expect(!any_nan(a0[0], a1[0]), 1)) break;
    bool any = false;
    FENCE();
#pragma unroll
    for (int k = 0; k < 8; k++)
      if (umax4(r[k]) == 0xffffffffu) { r[k] = __builtin_amdgcn_raw_buffer_load_b128(rws, voff + (STRIDE < 4096 ? (k & 3) * STRIDE : 0), STRIDE < 4096 ? (k >> 2) * 4 * STRIDE : k * STRIDE, SC1); any = true; }
    if (!__any(any) || spin >= SPIN_CAP) break;
  }
}
DEV void marks_wait(__amdgpu_buffer_rsrc_t rws, u32 voff, u32 tag) {
  for (u32 spin = 0; spin < SPIN_CAP; spin++) {
    FENCE();
    if (__all(__builtin_amdgcn_raw_buffer_load_b32(rws, voff, 0, SC1) == tag)) break;
  }
  FENCE();
}

__global__ void __launch_bounds__(320, 2)
fused_block(unsigned char* pws, const u16* __restrict__ pwq, const u16* __restrict__ pkc, const u16* __restrict__ pvc, const u16* __restrict__ px,
            const u16* __restrict__ pc, const u16* __restrict__ pwo, const u16* __restrict__ pw1, const u16* __restrict__ pwm, const u16* __restrict__ pb1,
            const u16* __restrict__ pw2, const u16* __restrict__ pb2, u16* __restrict__ pout) {
  __shared__ __attribute__((aligned(16))) char smem[163840];
  asm volatile("" ::"v"((LAS char*)smem));
  const int lane = threadIdx.x & 63, wave = __builtin_amdgcn_readfirstlane(threadIdx.x >> 6);
  const int wg = blockIdx.x, x = wg & 7, i = wg >> 3, b = i >> 3, s = i & 7;
  const int role = i & 3;
  const bool isM = role == 0, isO = role == 1 || role == 2, isF = role == 3 && x < 7;
  const int u = x * 8 + (i >> 2);
  const int oc = (i >> 2) * 2 + role - 1;

  if (__builtin_expect(wave == 4, 1)) {
    __builtin_amdgcn_s_setprio(3);
    if (lane < 16) *(volatile LAS u32*)(L_FLAG + 4 * lane) = 0u;
    asm volatile("" ::"s"(pws), "s"(pwq), "s"(pkc), "s"(pvc), "s"(pwo), "s"(pw1), "s"(pwm));
    u32 due = cyc(), pace = isM ? PACE_M : PACE_A;
    u32 hnext = due + HPOLL0;
    due += KSTART;
    STAMP(0);
    wg_barrier();
    if (isM) wg_barrier();
#define PACED(p) do { u32 now_; while ((int)((now_ = cyc()) - due) < 0) {} due = ((int)(now_ - due) > 2 * (p) ? now_ - 2 * (p) : due) + (p); } while (0)
    {
      const v4i rq = mkrs(pwq, 512 * 4096 * 2);
      const u32 vq = (role * 16 + (lane >> 3)) * 8192 + x * 1024 + (i >> 2) * 128 + (lane & 7) * 16;
#pragma unroll 1
      for (int j = 0; j < 4; j++) {
        PACED(pace);
#pragma unroll
        for (int n = 0; n < 4; n++) dma1(rq, vq, (j * 128 + (n >> 1) * 64 + (n & 1) * 8) * 8192, L_T1 + (j * 4 + n) * 1024);
      }
    }
    const u32 vo = ((b * 8 + x) * 8192 + 6144) * 256u + (lane >> 4) * 256 + 16 * ((lane & 15) ^ (4 * (lane >> 4)));
    const v4i rw4 = mkrs(pws, WS_END);
    const u32 vh = WS_H + role * 2048 + lane * 16;
    {
      u32 ep;
      asm volatile("s_load_dword %0, %1, 0x0\n\ts_waitcnt lgkmcnt(0)" : "=&s"(ep) : "s"(pws) : "memory");
      *(LAS v4u*)(L_HF + lane * 16) = v4u{ep, ep, ep, ep};
      *(LAS v4u*)(L_HF + 1024 + lane * 16) = v4u{ep, ep, ep, ep};
      FENCE();
    }
#define HPOLL() do { if ((int)(due - hnext) >= 0 && flag_get(F_H) == 0) { \
      asm volatile("s_mov_b32 m0, %3\n\ts_nop 0\n\tbuffer_load_dwordx4 %1, %0, %2 offen sc1 lds\n\tbuffer_load_dwordx4 %1, %0, %2 offen offset:1024 sc1 lds" \
                   ::"s"(rw4), "v"(vh), "s"(((due >> HSLOT) & 7) * 8192), "s"(L_HF) : "memory"); \
      hnext = (due | ((1u << HSLOT) - 1)) + 1; } } while (0)
#define CHUNK(j) ((j) * 32768 + ((s + (j) + x + 2 * b) & 7) * 4096)
    {
      const v4i rk = mkrs(pkc, 67108864);
#pragma unroll 1
      for (int j = 0; j < 16; j++) {
        PACED(pace);
        HPOLL();
        dma4(rk, vo, CHUNK(j), L_K + j * 4096);
        if (j == 3) {
          VMWAIT(16); flag_set(F_WQ, 1); STAMP(1);
          if (!isM) pace = PACE_B;
          const v4u ones = {~0u, ~0u, ~0u, ~0u};
          const u32 a0 = WS_O + (x * 32 + i) * 1024 + lane * 16;
          const u32 a1 = lane < 4 ? WS_ML + x * 1024 + lane * 256 + b * 64 + s * 8 : lane >= 32 && role != 3 ? (isO ? WS_X + x * 4096 + oc * 256 : WS_MODR + u * 256) + (lane - 32) * 8 : WS_END;
          asm volatile("buffer_store_dwordx4 %0, %2, %4, 0 offen sc1\n\tbuffer_store_dwordx4 %0, %2, %4, %5 offen sc1\n\t"
                       "buffer_store_dwordx2 %1, %3, %4, 0 offen sc1\n\ts_nop 1"
                       ::"v"(ones), "v"(v2u{~0u, ~0u}), "v"(a0), "v"(a1), "s"(rw4), "s"(WS_Y - WS_O) : "memory");
        }
        if (j == 8) { VMWAIT(20); flag_set(F_WQ, 2); STAMP(23); }
        VMWAIT(WIN - 4);
      }
    }
    {
      const v4i rv = mkrs(pvc, 67108864);
#pragma unroll 1
      for (int j = 0; j < 16; j++) {
        if (j == 12 && isM) stage_wait(1, lane);
        PACED(pace);
        HPOLL();
        dma4(rv, vo, CHUNK(j), L_V + j * 4096);
        if (j == 4) { VMWAIT(20); flag_set(F_K, 1); STAMP(2); }
        VMWAIT(WIN - 4);
      }
    }
    STAMP(3);
    for (u32 spin = 0; flag_get(F_H) == 0 && spin < SPIN_CAP; spin++) { PACED(pace); HPOLL(); VMWAIT(0); }
    flag_set(F_H, 1);
    const bool early = role != 3 && __all(*(volatile LAS u32*)(L_FLAG + 4 * (lane & 3)) >= 3u);
    if (!early) {
      VMWAIT(16); flag_set(F_V, 6); VMWAIT(8); flag_set(F_V, 7); VMWAIT(0); flag_set(F_V, 8); STAMP(4);
      if (role == 3) { if (!isF) return; due += F_DELAY; }
      stage_wait(3, lane);
    }
    const u32 stride = isO ? 1024 : isF ? 3584 : 6144;
    const v4i rl = isO ? mkrs(pwo, 4096 * 512 * 2) : isF ? mkrs(pw1, 512 * 1792 * 2) : mkrs(pwm, 512 * 3072 * 2);
    const u32 col0 = isO ? x * 512 * 1024 + oc * 64 : isF ? u * 64 : 2048 + u * 64;
    const u32 vl = (isO ? (lane >> 3) + 16 * ((lane >> 2) & 1) : lane >> 2) * stride + (lane & 3) * 16 + col0;
    const int half = 4 * ((isO ? oc : u) & 1);
#pragma unroll 1
    for (int g = 0; g < 8; g++) {
      if (g < 4) PACED(PACE_L);
#pragma unroll
      for (int n4 = 0; n4 < 4; n4++) {
        const int n = ((g + half) & 7) * 4 + n4;
        const u32 row = isO ? 128 * ((n >> 1) & 3) + 32 * (n >> 3) + 8 * (n & 1) : 16 * n;
        dma1(rl, vl, row * stride, L_K + n * 1024);
      }
      if (g == 1 && early) { VMWAIT(24); flag_set(F_V, 6); VMWAIT(16); flag_set(F_V, 7); VMWAIT(8); flag_set(F_V, 8); STAMP(4); }
    }
    VMWAIT(0);
    flag_set(F_LATE, 1);
    STAMP(5);
    return;
  }

  const int cw = wave, g = lane >> 4, c = lane & 15, q4 = (lane >> 2) & 3;
  const auto rws = __builtin_amdgcn_make_buffer_rsrc(pws, 0, WS_END, 0x00020000);
  const u32 trow = 128 * cw + 32 * g + q4;
  const u32 kel = 128 * cw + 32 * g + 8 * (c & 3);
  const u32 obase = WS_O + (x * 32 + i) * 1024 + cw * 256 + lane * 4;
  v4u sc_frag = {0, 0, 0, 0};
  u32 tag;
  if (isM) {
    const v4i rm = mkrs(pwm, 512 * 3072 * 2);
    const u32 vm = (lane >> 1) * 6144 + (lane & 1) * 1024 + u * 16;
#define MDMA(n) dma1(rm, vm, (4 * cw + ((u + (n)) & 3)) * 32 * 6144, L_T2 + (4 * cw + ((u + (n)) & 3)) * 1024)
    v4u cr, xr; u32 xe;
    asm volatile("global_load_dwordx4 %0, %3, %6\n\tglobal_load_dwordx4 %1, %4, %7\n\tglobal_load_dword %2, %5, %7"
                 : "=&v"(cr), "=&v"(xr), "=&v"(xe)
                 : "v"(((c >> 2) * 512 + kel) * 2), "v"(cw * 1024 + lane * 16), "v"((g * 512 + u * 8 + (c & 6)) * 2), "s"(pc), "s"(px)
                 : "memory");
    MDMA(0);
    __builtin_amdgcn_s_barrier();
    MDMA(1); MDMA(2); MDMA(3);
    __builtin_amdgcn_s_barrier();
    asm volatile("s_load_dword %3, %4, 0x0\n\ts_waitcnt vmcnt(4) lgkmcnt(0)" : "+v"(cr), "+v"(xr), "+v"(xe), "=&s"(tag) : "s"(pws) : "memory");
    tag += 1u;
#pragma unroll
    for (int e = 0; e < 4; e++) {
      const float lo = bflo(cr[e]), hi = bfhi(cr[e]);
      sc_frag[e] = pk(lo * __builtin_amdgcn_rcpf(1.f + fexp(-lo)), hi * __builtin_amdgcn_rcpf(1.f + fexp(-hi)));
    }
    v2f w[4] = {unpk(xr.x), unpk(xr.y), unpk(xr.z), unpk(xr.w)};
    const v2f t = (w[0] + w[1]) + (w[2] + w[3]);
    const float mean = wave_sum(t[0] + t[1]) * (1.f / 512.f);
    v2f sq = {0.f, 0.f};
#pragma unroll
    for (int j = 0; j < 4; j++) { const v2f d = w[j] - mean; sq += d * d; }
    const float den = rb(__builtin_amdgcn_sqrtf(rb(rb(wave_sum(sq[0] + sq[1]) * (1.f / 512.f)) + 1e-5f)));
    *(LAS v2f*)(L_LN + 8 * cw) = v2f{rb(mean), __builtin_amdgcn_rcpf(den)};
    PIN(sc_frag);
    VMWAIT(0);
    STAMP0(6);
    v4u wf[4];
#pragma unroll
    for (int k = 0; k < 4; k++) wf[k] = tr2(L_T2 + trow * 32 + 8 * (lane & 3) + 256 * k, L_T2 + trow * 32 + 8 * (lane & 3) + 256 * k + 128);
    *(LAS float*)(L_RM + lane * 16 + cw * 4) = dot_sets(sc_frag, wf);
    flag_set(cw, 1);
    if (cw == 0) {
      stage_wait(1, lane);
      const v4f p = *(LAS v4f*)(L_RM + lane * 16);
      const v2f ln = *(LAS v2f*)(L_LN + 8 * g);
      const float md = rb((p[0] + p[1]) + (p[2] + p[3]));
      const float h = rb(rb(rb(((c & 1) ? bfhi(xe) : bflo(xe)) - ln[0]) * ln[1]) * rb(1.f + dppf<ROR8>(md))) + md;
      const u32 hp = pk(h, dppf<QX1>(h));
      if ((lane & 9) == 0) *(LAS u32*)(L_RM + g * 16 + c * 2) = hp;
      FENCE();
      const v2u hq = *(LAS v2u*)(L_RM + (lane & 7) * 8);
      __builtin_amdgcn_raw_buffer_store_b128(v4u{hq.x, tag, hq.y, tag}, rws, WS_H + (lane >> 3) * 8192 + ((u >> 1) & 3) * 2048 + (x * 2 + (u & 1)) * 128 + (lane & 7) * 16, 0, SC1);
      STAMP(7);
    }
  } else {
    __builtin_amdgcn_s_barrier();
    asm volatile("s_load_dword %0, %1, 0x0\n\ts_waitcnt lgkmcnt(0)" : "=&s"(tag) : "s"(pws) : "memory");
    tag += 1u;
    if (i == 1) {
      unsigned long long pc_;
      asm volatile("s_getpc_b64 %0" : "=s"(pc_));
      u32 d_;
      asm volatile("global_load_dword %0, %1, %2\n\ts_waitcnt vmcnt(0)" : "=&v"(d_) : "v"(min((cw * 64 + lane) * 64, CODE_TAIL)), "s"(pc_) : "memory");
    }
    if (isF) {
      const v4u xr = *(const GAS v4u*)(px + cw * 512 + lane * 8);
      const v2f t = (unpk(xr.x) + unpk(xr.y)) + (unpk(xr.z) + unpk(xr.w));
      *(LAS float*)(L_LN + 4 * cw) = wave_sum(t[0] + t[1]) * (1.f / 512.f);
    }
  }
  u32 qdue;
  {
    v4u wf[4];
    flag_wait(F_WQ, 1);
#pragma unroll
    for (int k = 0; k < 4; k++) {
      const u32 a = L_T1 + (32 * g + q4) * 128 + cw * 32 + 8 * (lane & 3) + 1024 * k;
      wf[k] = tr2(a, a + 512);
    }
    PIN4(wf);
    u32 qo = WS_Q + (g * 8 + x) * 16384 + b * 4096 + role * 1024 + ((i >> 2) & 1) * 512 + cw * 128 + c * 8;
    u32 ha = L_HF + (4 * g + (c & 3)) * 128 + (c >> 2) * 32;
    PIN(qo); PIN(ha);
    flag_wait(F_WQ, 2);
    STAMP0(21);
    v4u r0, r1;
    for (;;) {
      FENCE();
      const u32 giveup = *(volatile LAS u32*)(L_FLAG + 4 * F_V);
      r0 = lds4(ha); r1 = lds4(ha + 16);
      if (__all(((r0.y ^ tag) | (r0.w ^ tag) | (r1.y ^ tag) | (r1.w ^ tag)) == 0u || giveup != 0u)) break;
    }
    flag_set(cw, 2);
    STAMP0(8);
    __builtin_amdgcn_raw_buffer_store_b64(v2u{__float_as_uint(dot_sets(v4u{r0.x, r0.z, r1.x, r1.z}, wf)), tag}, rws, qo, 0, SC1);
    qdue = cyc() + Q_DELAY;
    if (cw == 0) { stage_wait(2, lane); flag_set(F_H, 1); }
    STAMP0(9);
  }
  v4u kf[4][4];
  flag_wait(F_K, 1);
  {
    const u32 ka = L_K + cw * 16384 + 256 * c + 64 * (c & 3) + 16 * g;
#pragma unroll
    for (int j = 0; j < 4; j++)
#pragma unroll
      for (int t = 0; t < 4; t++) kf[j][t] = lds4((ka ^ (64 * t)) + 4096 * j);
  }
  flag_set(cw, 3);
  v4u pf[8];
  float oscale;
  {
    while ((int)(cyc() - qdue) < 0) {}
    STAMP0(22);
    {
      v4u r[4];
      for (u32 spin = 0; spin < SPIN_CAP; spin++) {
        FENCE();
        u32 bad = 0;
#pragma unroll
        for (int k = 0; k < 4; k++) r[k] = __builtin_amdgcn_raw_buffer_load_b128(rws, WS_Q + (b * 8 + x) * 16384 + cw * 4096 + lane * 16 + k * 1024, 0, SC1);
#pragma unroll
        for (int k = 0; k < 4; k++) bad |= (r[k].y ^ tag) | (r[k].w ^ tag);
        if (__all(bad == 0u)) break;
      }
      *(LAS u32*)(L_QL + cw * QL_S + lane * 4) = pk((__uint_as_float(r[0].x) + __uint_as_float(r[1].x)) + (__uint_as_float(r[2].x) + __uint_as_float(r[3].x)),
                                                    (__uint_as_float(r[0].z) + __uint_as_float(r[1].z)) + (__uint_as_float(r[2].z) + __uint_as_float(r[3].z)));
    }
    flag_set(cw, 4);
    stage_wait(4, lane);
    if (i == 1 && cw >= 2)
      __builtin_amdgcn_raw_buffer_store_b32(tag, rws, WS_GC + ((cw - 2) * 64 + lane) * 128 + x * 4, 0, SC1);
    STAMP0(10);
    const u32 qa = L_QL + (c >> 2) * QL_S + 16 * g;
    const v4u qf[4] = {lds4(qa), lds4(qa + 64), lds4(qa + 128), lds4(qa + 192)};
    v4f a[4];
#pragma unroll
    for (int t = 0; t < 4; t++)
#pragma unroll
      for (int j = 0; j < 4; j++) a[j] = t ? mfma(kf[j][t], qf[t], a[j]) : mfma0(kf[j][0], qf[0]);
    const v4f a01 = (c & 1) ? a[1] : a[0], a23 = (c & 1) ? a[3] : a[2], sa = (c & 2) ? a23 : a01;
    const float SC = 0.08838834764831845f;
    const v2f s0 = rb2(rb2(v2f{sa[0], sa[1]}) * SC), s1 = rb2(rb2(v2f{sa[2], sa[3]}) * SC);
    float m = fmx(fmx3(s0[0], s0[1], s1[0]), s1[1]);
    m = fmaxf(m, dppf<QX1>(m)); m = fmaxf(m, dppf<QX2>(m)); m = xg_max(m);
    *(LAS float*)(L_MX + (c >> 2) * 16 + cw * 4) = m;
    flag_set(cw, 5);
    stage_wait(5, lane);
    const v4f mx = *(LAS v4f*)(L_MX + (c >> 2) * 16), mh = *(LAS v4f*)(L_MX + g * 16);
    m = fmx(fmx3(mx[0], mx[1], mx[2]), mx[3]);
    const float p0 = fexp(s0[0] - m), p1 = fexp(s0[1] - m), p2 = fexp(s1[0] - m), p3 = fexp(s1[1] - m);
    const u32 pa = L_PL + (c >> 2) * 64 + g * 16;
    *(LAS v2u*)(pa + (2 * cw + ((c >> 1) & 1)) * 256 + (c & 1) * 8) = v2u{pk(p0, p1), pk(p2, p3)};
    *(LAS float*)(L_SM + (c >> 2) * 16 + cw * 4) = xg_sum(quad_sum((p0 + p1) + (p2 + p3)));
    flag_set(cw, 6);
    stage_wait(6, lane);
    const v4f sm = *(LAS v4f*)(L_SM + g * 16);
    const float ml2 = fmx(fmx3(mh[0], mh[1], mh[2]), mh[3]) * 1.4426950408889634f, me = __builtin_ceilf(ml2);
    oscale = __builtin_amdgcn_exp2f(ml2 - me);
    if (cw == 0 && c == 0)
      __builtin_amdgcn_raw_buffer_store_b64(v2u{nosent(__float_as_uint(me)), nosent(__float_as_uint(((sm[0] + sm[1]) + (sm[2] + sm[3])) * oscale))}, rws,
                                            WS_ML + x * 1024 + g * 256 + b * 64 + s * 8, 0, SC1);
#pragma unroll
    for (int k = 0; k < 8; k++) pf[k] = lds4(pa + k * 256);
    PIN8(pf);
    PIN(oscale);
    STAMP0(12);
  }
  {
    const u32 va = L_V + 256 * (4 * g + q4) + 64 * (cw ^ q4) + 8 * (lane & 3);
    v4f o0, o1;
    flag_wait(F_V, 6);
    {
      v4u vf[6][2];
#pragma unroll
      for (int k = 0; k < 6; k++) { vf[k][0] = tr2(va + 8192 * k, va + 8192 * k + 4096); vf[k][1] = tr2(va + 8192 * k + 32, va + 8192 * k + 4096 + 32); }
      SCHED();
      o0 = mfma0(pf[0], vf[0][0]); o1 = mfma0(pf[0], vf[0][1]);
#pragma unroll
      for (int k = 1; k < 6; k++) { o0 = mfma(pf[k], vf[k][0], o0); o1 = mfma(pf[k], vf[k][1], o1); }
    }
#pragma unroll
    for (int k = 6; k < 8; k++) {
      flag_wait_hot(F_V, k + 1);
      o0 = mfma(pf[k], tr2(va + 8192 * k, va + 8192 * k + 4096), o0);
      o1 = mfma(pf[k], tr2(va + 8192 * k + 32, va + 8192 * k + 4096 + 32), o1);
    }
    __builtin_amdgcn_raw_buffer_store_b32(nosent(pk(o0[0] * oscale, o1[0] * oscale)), rws, obase, 0, SC1);
  }
  STAMP0(13);
  if (isM | isO | isF) {
    const u32 hm = (c & 1) ? 0xffff0000u : 0x0000ffffu, mk0 = (c & 2) ? 0u : hm, mk1 = (c & 2) ? hm : 0u;
    const u32 ta = L_K + trow * 64 + 8 * (lane & 3), rw = L_RED + ((cw * 4 + g) * 32 + c) * 4;
    v4u wa[4], wb[4];
    if (!isF) {
      v4u af = sc_frag;
      v4u r[8];
      v2u w[8];
      float linv = 0.f;
      const u32 og = WS_O + (x * 32 + (c >> 2) * 8) * 1024 + cw * 256 + g * 64 + (c & 3) * 16;
      if (isO) {
        v4u ml = __builtin_amdgcn_raw_buffer_load_b128(rws, WS_ML + x * 1024 + lane * 16, 0, SC1);
        gather_issue<1024>(rws, og, r);
        for (u32 spin = 0; __any(umax4(ml) == 0xffffffffu) && spin < SPIN_CAP; spin++) { FENCE(); ml = __builtin_amdgcn_raw_buffer_load_b128(rws, WS_ML + x * 1024 + lane * 16, 0, SC1); }
        float M = fmx(__uint_as_float(ml.x), __uint_as_float(ml.z));
        M = fmaxf(M, dppf<QX1>(M)); M = fmaxf(M, dppf<QX2>(M));
        const float w0 = __builtin_amdgcn_exp2f(__uint_as_float(ml.x) - M), w1 = __builtin_amdgcn_exp2f(__uint_as_float(ml.z) - M);
        linv = __builtin_amdgcn_rcpf(quad_sum(w0 * __uint_as_float(ml.y) + w1 * __uint_as_float(ml.w)));
        const u32 p0 = pk(w0, w0), p1 = pk(w1, w1);
        w[0] = v2u{dppu<0x00>(p0) & mk0, dppu<0x00>(p0) & mk1}; w[1] = v2u{dppu<0x00>(p1) & mk0, dppu<0x00>(p1) & mk1};
        w[2] = v2u{dppu<0x55>(p0) & mk0, dppu<0x55>(p0) & mk1}; w[3] = v2u{dppu<0x55>(p1) & mk0, dppu<0x55>(p1) & mk1};
        w[4] = v2u{dppu<0xAA>(p0) & mk0, dppu<0xAA>(p0) & mk1}; w[5] = v2u{dppu<0xAA>(p1) & mk0, dppu<0xAA>(p1) & mk1};
        w[6] = v2u{dppu<0xFF>(p0) & mk0, dppu<0xFF>(p0) & mk1}; w[7] = v2u{dppu<0xFF>(p1) & mk0, dppu<0xFF>(p1) & mk1};
        STAMP0(11);
      }
      flag_wait_hot(F_LATE, 1);
#pragma unroll
      for (int k = 0; k < 4; k++) { wa[k] = tr2(ta + 512 * k, ta + 512 * k + 256); wb[k] = tr2(ta + 512 * k + 32, ta + 512 * k + 32 + 256); }
      PIN4(wa); PIN4(wb);
      STAMP0(14);
      if (isO) {
        v4f a0, a1;
        gather_sum<1024>(rws, og, r, w, a0, a1);
        STAMP0(16);
        a0 *= linv; a1 *= linv;
        af = v4u{pk(a0[0], a0[1]), pk(a0[2], a0[3]), pk(a1[0], a1[1]), pk(a1[2], a1[3])};
      }
      *(LAS float*)rw = dot_sets(af, wa);
      *(LAS float*)(rw + 64) = dot_sets(af, wb);
      wg_barrier();
      if (cw == 0) {
        const u32 ra = L_RED + lane * 8;
        const v2f t = (*(LAS v2f*)ra + *(LAS v2f*)(ra + 512)) + (*(LAS v2f*)(ra + 1024) + *(LAS v2f*)(ra + 1536));
        __builtin_amdgcn_raw_buffer_store_b32(nosent(pk2(t)), rws, (isO ? WS_X + x * 4096 + oc * 256 : WS_MODR + u * 256) + lane * 4, 0, SC1);
        STAMP(17);
      } else if (cw == 1 && isM) {
        const int ub = u >> 4, ct = u & 15, el = 8 * (c & 3) + 4 * (g & 1);
        const v2u xv = *(const GAS v2u*)(px + ub * 512 + ct * 32 + el), b2v = *(const GAS v2u*)(pb2 + ct * 32 + el);
        const u32 one = 0x3F803F80u & hm;
        const v4u sl = {(c >> 1) == 0 ? one : 0u, (c >> 1) == 1 ? one : 0u, (c >> 1) == 2 ? one : 0u, (c >> 1) == 3 ? one : 0u};
        marks_wait(rws, WS_GC + u * 128 + (lane & 7) * 4, tag);
        v2u gav, gbv; v4u xp = {0, 0, 0, 0};
        for (u32 spin = 0; spin < SPIN_CAP; spin++) {
          FENCE();
          gav = __builtin_amdgcn_raw_buffer_load_b64(rws, WS_MODR + ct * 256 + ub * 64 + el * 2, 0, SC1);
          gbv = __builtin_amdgcn_raw_buffer_load_b64(rws, WS_MODR + 12288 + ct * 256 + ub * 64 + el * 2, 0, SC1);
          if (lane < 32) xp = __builtin_amdgcn_raw_buffer_load_b128(rws, WS_X + (lane >> 2) * 4096 + ct * 256 + ub * 64 + (lane & 3) * 16, 0, SC1);
          if (__all(max(max(max(gav.x, gav.y), max(gbv.x, gbv.y)), umax4(xp)) != 0xffffffffu)) break;
          __builtin_amdgcn_s_sleep(1);
        }
        v4f at = mfma0(sl, xp);
#pragma unroll
        for (int r = 0; r < 4; r++) { at[r] += dppf<ROR4>(at[r]); at[r] += dppf<ROR8>(at[r]); }
        v2f x10 = rb2(unpk(xv.x) + rb2(unpk(gav.x) * rb2(v2f{at[0], at[1]}))), x11 = rb2(unpk(xv.y) + rb2(unpk(gav.y) * rb2(v2f{at[2], at[3]})));
        v2f g0 = unpk(gbv.x), g1 = unpk(gbv.y);
        x10 += g0 * unpk(b2v.x); x11 += g1 * unpk(b2v.y);
        PIN(x10); PIN(x11); PIN(g0); PIN(g1);
        STAMP(18);
        v4u y[4];
        v4f ys;
        y[3] = v4u{0, 0, 0, 0};
        for (u32 spin = 0;; spin++) {
          FENCE();
#pragma unroll
          for (int k = 0; k < 3; k++) y[k] = __builtin_amdgcn_raw_buffer_load_b128(rws, WS_Y + u * 4096 + k * 1024 + lane * 16, 0, SC1);
          if (lane < 32) y[3] = __builtin_amdgcn_raw_buffer_load_b128(rws, WS_Y + u * 4096 + 3072 + lane * 16, 0, SC1);
          ys = mfma0(sl, y[0]); ys = mfma(sl, y[1], ys); ys = mfma(sl, y[2], ys); ys = mfma(sl, y[3], ys);
          if (__builtin_expect(!any_nan(ys[0], ys[0]), 1)) break;
          if (__all(max(max(umax4(y[0]), umax4(y[1])), max(umax4(y[2]), umax4(y[3]))) != 0xffffffffu) || spin >= SPIN_CAP) break;
        }
        STAMP(19);
#pragma unroll
        for (int r = 0; r < 4; r++) { ys[r] += dppf<ROR4>(ys[r]); ys[r] += dppf<ROR8>(ys[r]); }
        if (lane < 32 && c < 4) *(GAS v2u*)(pout + ub * 512 + ct * 32 + el) = v2u{pk2(x10 + g0 * v2f{ys[0], ys[1]}), pk2(x11 + g1 * v2f{ys[2], ys[3]})};
        if (u == 0 && lane == 0) __builtin_amdgcn_raw_buffer_store_b32(tag, rws, WS_EPOCH, 0, SC1);
        STAMP(20);
      }
    } else {
      v4u w2f[8];
      {
        v4u w2r[8];
        const auto r2 = __builtin_amdgcn_make_buffer_rsrc((void*)pw2, 0, 1792 * 512 * 2, 0x00020000);
        const u32 o = (u * 32 + 8 * g) * 1024 + cw * 256 + c * 16;
#pragma unroll
        for (int r = 0; r < 8; r++) w2r[r] = __builtin_amdgcn_raw_buffer_load_b128(r2, o + (r & 3) * 1024, (r >> 2) * 4096, NT);
#pragma unroll
        for (int j = 0; j < 8; j++) {
#pragma unroll
          for (int e = 0; e < 4; e++) w2f[j][e] = __builtin_amdgcn_perm(w2r[2 * e + 1][j >> 1], w2r[2 * e][j >> 1], (j & 1) ? 0x07060302u : 0x05040100u);
          PIN(w2f[j]);
        }
      }
      const u32 b1p = *(const GAS u32*)(pb1 + u * 32 + 8 * g + 2 * (c & 3));
      const v4u xq = *(const GAS v4u*)(px + (c >> 2) * 512 + kel);
      flag_wait(F_LATE, 1);
#pragma unroll
      for (int k = 0; k < 4; k++) { wa[k] = tr2(ta + 512 * k, ta + 512 * k + 256); wb[k] = tr2(ta + 512 * k + 32, ta + 512 * k + 32 + 256); }
      PIN4(wa); PIN4(wb);
      v4u gq, sf, cq;
      marks_wait(rws, WS_GC + (64 + u) * 128 + (lane & 7) * 4, tag);
      for (u32 spin = 0; spin < SPIN_CAP; spin++) {
        FENCE();
        gq = __builtin_amdgcn_raw_buffer_load_b128(rws, WS_MODR + cw * 1024 + lane * 16, 0, SC1);
        sf = __builtin_amdgcn_raw_buffer_load_b128(rws, WS_MODR + cw * 1024 + lane * 16, 4096, SC1);
        cq = __builtin_amdgcn_raw_buffer_load_b128(rws, WS_MODR + cw * 1024 + lane * 16, 8192, SC1);
        if (__all(max(max(umax4(gq), umax4(sf)), umax4(cq)) != 0xffffffffu)) break;
      }
      STAMP0(15);
      const float piv = *(LAS float*)(L_LN + (c >> 2) * 4);
      v4u gf;
      v2f ga[4], xz[4], gg[4], xg[4];
#pragma unroll
      for (int e = 0; e < 4; e++) {
        gf[e] = pk2(unpk(cq[e]) + 1.f);
        ga[e] = unpk(gq[e]); xz[e] = unpk(xq[e]) - piv; gg[e] = ga[e] * unpk(gf[e]); xg[e] = xz[e] * unpk(gf[e]);
        PIN(ga[e]); PIN(xz[e]); PIN(gg[e]); PIN(xg[e]);
      }
      *(LAS float*)(rw - L_RED + L_TS) = dot_sets(gf, wa);
      *(LAS float*)(rw - L_RED + L_TS + 64) = dot_sets(gf, wb);
      *(LAS float*)(rw - L_RED + L_TS + 2048) = dot_sets(sf, wa);
      *(LAS float*)(rw - L_RED + L_TS + 2048 + 64) = dot_sets(sf, wb);
      wg_barrier();
      const u32 ra = ((c >> 2) * 32 + 8 * g + 2 * (c & 3)) * 4;
      v2f T = (*(LAS v2f*)(L_TS + ra) + *(LAS v2f*)(L_TS + ra + 512)) + (*(LAS v2f*)(L_TS + ra + 1024) + *(LAS v2f*)(L_TS + ra + 1536));
      v2f S = (*(LAS v2f*)(L_TS + ra + 2048) + *(LAS v2f*)(L_TS + ra + 2560)) + (*(LAS v2f*)(L_TS + ra + 3072) + *(LAS v2f*)(L_TS + ra + 3584)) + unpk(b1p);
      PIN(T); PIN(S);
      v4f a0, a1;
      v4u r[8];
      const v2u id = {0x3F803F80u & mk0, 0x3F803F80u & mk1}, w[8] = {id, id, id, id, id, id, id, id};
      gather_issue<4096>(rws, WS_X + cw * 1024 + lane * 16, r);
      gather_sum<4096>(rws, WS_X + cw * 1024 + lane * 16, r, w, a0, a1);
      STAMP0(16);
      const v2f at[4] = {v2f{a0[0], a0[1]}, v2f{a0[2], a0[3]}, v2f{a1[0], a1[1]}, v2f{a1[2], a1[3]}};
      const v4u af = {pk2(at[0] * gg[0] + xg[0]), pk2(at[1] * gg[1] + xg[1]), pk2(at[2] * gg[2] + xg[2]), pk2(at[3] * gg[3] + xg[3])};
      const float da = dot_sets(af, wa), db = dot_sets(af, wb);
      v2f s1 = {0.f, 0.f}, s2 = {0.f, 0.f};
#pragma unroll
      for (int e = 0; e < 4; e++) { const v2f z = at[e] * ga[e] + xz[e]; s1 += z; s2 += z * z; }
      *(LAS v2f*)(L_ST + (c >> 2) * 32 + cw * 8) = v2f{xg_sum(quad_sum(s1[0] + s1[1])), xg_sum(quad_sum(s2[0] + s2[1]))};
      *(LAS float*)rw = da;
      *(LAS float*)(rw + 64) = db;
      wg_barrier();
      const v4f st0 = *(LAS v4f*)(L_ST + (c >> 2) * 32), st1 = *(LAS v4f*)(L_ST + (c >> 2) * 32 + 16);
      const v2f P = (*(LAS v2f*)(L_RED + ra) + *(LAS v2f*)(L_RED + ra + 512)) + (*(LAS v2f*)(L_RED + ra + 1024) + *(LAS v2f*)(L_RED + ra + 1536));
      const v2f st = ((v2f{st0[0], st0[1]} + v2f{st0[2], st0[3]}) + (v2f{st1[0], st1[1]} + v2f{st1[2], st1[3]})) * (1.f / 512.f);
      const float rs = __builtin_amdgcn_rsqf(fmaxf(st[1] - st[0] * st[0], 0.f) + 1e-5f);
      const v2f pre = (P - T * st[0]) * rs + S;
      const float h0 = pre[0] > 0.f ? pre[0] : 0.f, h1 = pre[1] > 0.f ? pre[1] : 0.f;
      const u32 hp = pk(h0 * h0, h1 * h1);
      const v4u hf = {dppu<0x00>(hp), dppu<0x55>(hp), dppu<0xAA>(hp), dppu<0xFF>(hp)};
      float y[8];
#pragma unroll
      for (int j = 0; j < 8; j++) y[j] = mfma0(hf, w2f[j])[0];
      __builtin_amdgcn_raw_buffer_store_b128(v4u{nosent(pk(y[0], y[1])), nosent(pk(y[2], y[3])), nosent(pk(y[4], y[5])), nosent(pk(y[6], y[7]))}, rws,
                                             WS_Y + (g * 16 + 4 * cw + (c >> 2)) * 4096 + u * 64 + (c & 3) * 16, 0, SC1);
      STAMP0(17);
    }
  }
  if (cw == (isM ? 1 : 0)) {
    __builtin_amdgcn_s_sleep(64);
    const u32 t0_ = __builtin_amdgcn_readfirstlane(*(volatile LAS u32*)(L_STAMP));
    u32 d_ = __builtin_amdgcn_readfirstlane(*(volatile LAS u32*)(L_STAMP + 4 * (LEAK & 31))) - t0_;
    if (LEAK == 100) { d_ = 0; if (isM) { for (int w_ = 0; w_ < 8; w_++) { u32 o_ = __builtin_amdgcn_readfirstlane(__hip_atomic_load((GAS u32*)(pws + WS_STAMP) + w_, __ATOMIC_RELAXED, __HIP_MEMORY_SCOPE_AGENT)); int df = (int)(t0_ - o_); if (df > (int)d_) d_ = df; } } }
    if (d_ > 4000) d_ = 4000;
    while ((int)(rtv() - (t0_ + 4000 + 4 * d_)) < 0) __builtin_amdgcn_s_sleep(1);
  }

}

extern "C" void kernel_launch(void* const* d_in, const int* in_sizes, int n_in, void* d_out, int out_size, void* d_ws, size_t ws_size, hipStream_t stream) {
  fused_block<<<256, 320, 0, stream>>>((unsigned char*)d_ws, (const u16*)d_in[3], (const u16*)d_in[1], (const u16*)d_in[2], (const u16*)d_in[0], (const u16*)d_in[10],
                                       (const u16*)d_in[4], (const u16*)d_in[6], (const u16*)d_in[11], (const u16*)d_in[7], (const u16*)d_in[8], (const u16*)d_in[9],
                                       (u16*)d_out);
}
```

```cpp
#include <hip/hip_runtime.h>
#include <hip/hip_bf16.h>
#pragma clang diagnostic ignored "-Wint-to-pointer-cast"

typedef unsigned u32;
typedef unsigned short u16;
typedef u32 v4u __attribute__((ext_vector_type(4)));
typedef u32 v2u __attribute__((ext_vector_type(2)));
typedef int v4i __attribute__((ext_vector_type(4)));
typedef float v4f __attribute__((ext_vector_type(4)));
typedef float v2f __attribute__((ext_vector_type(2)));
typedef short v4s __attribute__((ext_vector_type(4)));
typedef __bf16 v8bf __attribute__((ext_vector_type(8)));
typedef __bf16 v2bf __attribute__((ext_vector_type(2)));
typedef __amdgpu_buffer_rsrc_t rsrc_t;
#define LAS __attribute__((address_space(3)))
#define GAS __attribute__((address_space(1)))
#define DEV __device__ __forceinline__
#define FENCE() asm volatile("" ::: "memory")
#define SCHED() __builtin_amdgcn_sched_barrier(0)
#define PIN(v) asm volatile("" : "+v"(v))
#define PIN4(a) asm volatile("" : "+v"(a[0]), "+v"(a[1]), "+v"(a[2]), "+v"(a[3]))
#define PIN8(a) asm volatile("" : "+v"(a[0]), "+v"(a[1]), "+v"(a[2]), "+v"(a[3]), "+v"(a[4]), "+v"(a[5]), "+v"(a[6]), "+v"(a[7]))
#define LIKELY(x) __builtin_expect(!!(x), 1)

#define TP(k, d) (d)
#define T_END TP(0, 9850)
#define T_H TP(1, 4400)
#define H_PER TP(2, 1200)
#define T_Q TP(3, 7400)
#define T_FAST TP(4, 100)
#define Q_DELAY 300
#define T_LO TP(5, 9940)
#define LO_GAP TP(6, 500)
#define LO_GAP2 TP(7, 300)
#define T_W TP(8, 10830)
#define T_LF TP(9, 11300)
#define T_G TP(10, 12100)
#define T_X TP(11, 13900)
#define Y_DELAY TP(12, 1000)
#define STAG TP(13, 70)
#define T_GC TP(14, 9700)
#define Y_OFF TP(15, 2100)
#define G_MIN 150
#define T_S TP(16, 1800)
#define W2_GAP TP(17, 1400)
#define LF_GAP TP(22, 1400)
#define X_PREP TP(23, 200)
#define STAG_E 0
#define ML_OFF TP(18, 1100)
#define PACE_MIN 280
#define KSTART TP(19, 220)
#define T_RP TP(20, 120)
#define C_START TP(21, 1000)
constexpr int WIN = 40;

constexpr u32 PS = 2304, LS = 2176;
constexpr u32 WS_EPOCH = 0, WS_GC = 4096, WS_ST = WS_GC + 1024, WS_ML = 32768, WS_O = 65536, WS_Q = WS_O + 262144, WS_Y = WS_Q + 524288, WS_MODR = WS_Y + 262144, WS_X = WS_MODR + 64 * PS,
              WS_H = WS_X + 128 * PS, WS_END = WS_H + 512 * LS;
constexpr u32 WS_STAMP = 1u << 22;
constexpr u32 L_FLAG = 64, L_MX = 192, L_SM = 256, L_LN = 320, L_ST = 15872, L_STAMP = 512, L_HF = 1024, L_ML = 3072, L_RM = 5120, L_RED = 6144, L_TS = 8192, L_PL = 12288,
              L_QL = 14336, QL_S = 288, L_JUNK = 15616, L_T1 = 16384, L_K = 32768, L_V = 98304, L_T2 = 131072;
enum { F_K = 4, F_V, F_LATE, F_WQ, F_TAG, F_T, F_GU, F_TI, F_T0, F_CF, F_TR };
#define CODE_TAIL 20392
constexpr u32 SPIN_CAP = 1u << 18;
constexpr int SC1 = 16;

DEV u32 rtv() { unsigned long long t; asm volatile("s_memrealtime %0\n\ts_waitcnt lgkmcnt(0)" : "=s"(t)::"memory"); return (u32)t; }
DEV u32 now() { return rtv() << 4; }
DEV void until(u32 t) { while ((int)(now() - t) < 0) {} }
DEV void until_idle(u32 t) { while ((int)(now() - t) < 0) __builtin_amdgcn_s_sleep(1); }
#define STAMP(k)
#define STAMP0(k)

DEV float bflo(u32 v) { return __uint_as_float(v << 16); }
DEV float bfhi(u32 v) { return __uint_as_float(v & 0xffff0000u); }
DEV v2f unpk(u32 v) { return v2f{bflo(v), bfhi(v)}; }
DEV u32 pk2(v2f p) { return __builtin_bit_cast(u32, __builtin_convertvector(p, v2bf)); }
DEV u32 pk(float lo, float hi) { return pk2(v2f{lo, hi}); }
DEV float rb(float f) { return bflo(pk(f, f)); }
DEV v2f rb2(v2f p) { return unpk(pk2(p)); }
DEV float fmx(float a, float b) { return __builtin_amdgcn_fmed3f(a, b, __builtin_inff()); }
DEV float fexp(float a) { return __builtin_amdgcn_exp2f(a * 1.4426950408889634f); }
DEV u32 nosent(u32 v) { return v < 0xfffffffeu ? v : 0xfffffffeu; }
DEV v4f mfma(v4u a, v4u b, v4f c) { return __builtin_amdgcn_mfma_f32_16x16x32_bf16(__builtin_bit_cast(v8bf, a), __builtin_bit_cast(v8bf, b), c, 0, 0, 0); }
DEV v4f mfma0(v4u a, v4u b) { return mfma(a, b, v4f{0.f, 0.f, 0.f, 0.f}); }
DEV v2u trr(u32 addr) { return __builtin_bit_cast(v2u, __builtin_amdgcn_ds_read_tr16_b64_v4i16((LAS v4s*)addr)); }
DEV v4u tr2(u32 a0, u32 a1) { v2u a = trr(a0), b = trr(a1); return v4u{a.x, a.y, b.x, b.y}; }
DEV v4u lds4(u32 addr) { return *(LAS v4u*)addr; }
DEV v4u lds4v(u32 addr) { return *(volatile LAS v4u*)addr; }
DEV v2f lds2f(u32 addr) { return *(LAS v2f*)addr; }
DEV v2f sum4(u32 a) { return (lds2f(a) + lds2f(a + 512)) + (lds2f(a + 1024) + lds2f(a + 1536)); }
template <int C> DEV float dppf(float x) { return __builtin_bit_cast(float, __builtin_amdgcn_mov_dpp(__builtin_bit_cast(int, x), C, 0xf, 0xf, true)); }
template <int C> DEV u32 dppu(u32 x) { return (u32)__builtin_amdgcn_mov_dpp((int)x, C, 0xf, 0xf, true); }
constexpr int QX1 = 0xB1, QX2 = 0x4E, ROR4 = 0x124, ROR8 = 0x128;
DEV float xg_sum(float x) {
  auto s = __builtin_amdgcn_permlane16_swap(__float_as_uint(x), __float_as_uint(x), false, false);
  x = __uint_as_float(s[0]) + __uint_as_float(s[1]);
  auto t = __builtin_amdgcn_permlane32_swap(__float_as_uint(x), __float_as_uint(x), false, false);
  return __uint_as_float(t[0]) + __uint_as_float(t[1]);
}
DEV float qg_max(float a, float b, float c, float d) {
  float m, t;
  asm("v_max3_f32 %0, %2, %3, %4\n\tv_max_f32 %0, %0, %5\n\ts_nop 1\n\tv_max_f32_dpp %0, %0, %0 quad_perm:[1,0,3,2] row_mask:0xf bank_mask:0xf\n\ts_nop 1\n\t"
      "v_max_f32_dpp %0, %0, %0 quad_perm:[2,3,0,1] row_mask:0xf bank_mask:0xf\n\tv_mov_b32 %1, %0\n\ts_nop 1\n\tv_permlane16_swap_b32 %0, %1\n\ts_nop 0\n\tv_max_f32 %0, %0, %1\n\t"
      "v_mov_b32 %1, %0\n\ts_nop 1\n\tv_permlane32_swap_b32 %0, %1\n\ts_nop 0\n\tv_max_f32 %0, %0, %1"
      : "=&v"(m), "=&v"(t) : "v"(a), "v"(b), "v"(c), "v"(d));
  return m;
}
DEV float quad_sum(float x) { x += dppf<QX1>(x); return x + dppf<QX2>(x); }
DEV float wave_sum(float x) { x = quad_sum(x); x += dppf<ROR4>(x); x += dppf<ROR8>(x); return xg_sum(x); }
DEV u32 umax4(v4u v) { return max(max(v.x, v.y), max(v.z, v.w)); }
DEV bool unset(v4u v) { return umax4(v) == 0xffffffffu; }
DEV u32 untagged(v4u v, u32 tag) { return (v.y ^ tag) | (v.w ^ tag); }
DEV bool any_nan(float a, float b) { return __any(__builtin_isunordered(a, b)); }
DEV v4u ld16(rsrc_t r, u32 v, u32 s) { return __builtin_amdgcn_raw_buffer_load_b128(r, v, s, SC1); }
DEV void st4(rsrc_t r, u32 d, u32 v) { __builtin_amdgcn_raw_buffer_store_b32(d, r, v, 0, SC1); }
DEV void st8(rsrc_t r, v2u d, u32 v) { __builtin_amdgcn_raw_buffer_store_b64(d, r, v, 0, SC1); }
DEV void st16(rsrc_t r, v4u d, u32 v) { __builtin_amdgcn_raw_buffer_store_b128(d, r, v, 0, SC1); }

DEV v4i mkrs(const void* p, u32 bytes) { v4i r; r.x = (int)(size_t)p; r.y = (int)((size_t)p >> 32) & 0xffff; r.z = (int)bytes; r.w = 0x00020000; return r; }
#define DMA4V(POL, rs, v0, v1, v2, v3, soff, lds) \
  asm volatile("s_mov_b32 m0, %6\n\ts_nop 0\n\tbuffer_load_dwordx4 %1, %0, %5 offen " POL " lds\n\tbuffer_load_dwordx4 %2, %0, %5 offen offset:1024 " POL " lds\n\t" \
               "buffer_load_dwordx4 %3, %0, %5 offen offset:2048 " POL " lds\n\tbuffer_load_dwordx4 %4, %0, %5 offen offset:3072 " POL " lds" \
               ::"s"(rs), "v"(v0), "v"(v1), "v"(v2), "v"(v3), "s"(soff), "s"(lds) : "memory")
#define DMA2V(POL, rs, v0, v1, soff, lds) \
  asm volatile("s_mov_b32 m0, %4\n\ts_nop 0\n\tbuffer_load_dwordx4 %1, %0, %3 offen " POL " lds\n\tbuffer_load_dwordx4 %2, %0, %3 offen offset:1024 " POL " lds" \
               ::"s"(rs), "v"(v0), "v"(v1), "s"(soff), "s"(lds) : "memory")
#define TOUCH(o, p) asm volatile("s_mov_b32 m0, %2\n\ts_nop 0\n\tglobal_load_lds_dword %0, %1" ::"v"(o), "s"(p), "i"(L_JUNK) : "memory")
#define VMWAIT(n) asm volatile("s_waitcnt vmcnt(%0)" ::"i"(n) : "memory")
DEV u32 flag_raw(int f) { return *(volatile LAS u32*)(L_FLAG + 4 * f); }
DEV u32 flag_get(int f) { return __builtin_amdgcn_readfirstlane(flag_raw(f)); }
DEV void flag_set(int f, u32 v) { FENCE(); *(volatile LAS u32*)(L_FLAG + 4 * f) = v; FENCE(); }
DEV void flag_wait(int f, u32 v) { while (flag_get(f) < v) __builtin_amdgcn_s_sleep(1); FENCE(); }
DEV void flag_wait_hot(int f, u32 v) { while (flag_get(f) < v) {} FENCE(); }
DEV u32 stage_raw(int lane) { return *(volatile LAS u32*)(L_FLAG + 4 * (lane & 3)); }
DEV void stage_wait(u32 v, int lane) { while (!__all(stage_raw(lane) >= v)) {} FENCE(); }
DEV void wg_barrier() { asm volatile("s_waitcnt lgkmcnt(0)\n\ts_barrier" ::: "memory"); }
DEV u32 load_tag(const void* pws) {
  u32 e;
  asm volatile("s_load_dword %0, %1, 0x0\n\ts_waitcnt lgkmcnt(0)" : "=&s"(e) : "s"(pws) : "memory");
  return e + (e == ~0u ? 2u : 1u);
}

DEV float dot16(v4u a, const v4u (&w)[4]) {
  v4f d[4] = {mfma0(a, w[0]), mfma0(a, w[1]), mfma0(a, w[2]), mfma0(a, w[3])};
  SCHED();
  return (d[0][0] + d[1][1]) + (d[2][2] + d[3][3]);
}
DEV v2f dot32(v4u a, const v4u (&w)[8]) {
  v4f d[8];
#pragma unroll
  for (int k = 0; k < 8; k++) d[k] = mfma0(a, w[k]);
  SCHED();
  float p0 = d[0][0] + d[2][1], p1 = d[1][0] + d[3][1];
  PIN(p0); PIN(p1);
  p0 += d[4][2]; p1 += d[5][2];
  PIN(p0); PIN(p1);
  return v2f{p0 + d[6][3], p1 + d[7][3]};
}
DEV void tile32(u32 ta, v4u (&w)[8]) {
#pragma unroll
  for (int k = 0; k < 8; k++) w[k] = tr2(ta + 512 * (k >> 1) + 32 * (k & 1), ta + 512 * (k >> 1) + 32 * (k & 1) + 256);
}
DEV void red_put(u32 a, v2f v) { *(LAS float*)a = v[0]; *(LAS float*)(a + 64) = v[1]; }
DEV v4f mfma4(v2u a, u32 b0, u32 b1, v4f c) {
  return __builtin_amdgcn_mfma_f32_4x4x4bf16_1k(__builtin_bit_cast(v4s, a), __builtin_bit_cast(v4s, v2u{b0, b1}), c, 0, 0, 0);
}
template <int S> DEV v4u gld(rsrc_t r, u32 v, int k) { return S < 4096 ? ld16(r, v + (k & 3) * S, (k >> 2) * 4 * S) : ld16(r, v, k * S); }
DEV void wsum8(const v4u (&r)[8], const v2u (&w)[8], v4f& a0, v4f& a1) {
  a0 = v4f{0.f, 0.f, 0.f, 0.f}; a1 = a0;
#pragma unroll
  for (int k = 0; k < 8; k++) { a0 = mfma4(w[k], r[k].x, r[k].y, a0); a1 = mfma4(w[k], r[k].z, r[k].w, a1); }
}
template <int S> DEV void reread8(rsrc_t rws, u32 v, v4u (&r)[8]) {
  for (u32 spin = 0; spin < SPIN_CAP; spin++) {
    bool b[8], any = false;
#pragma unroll
    for (int k = 0; k < 8; k++) { b[k] = unset(r[k]); any |= b[k]; }
    if (!__any(any)) break;
    FENCE();
#pragma unroll
    for (int k = 0; k < 8; k++)
      if (b[k]) r[k] = gld<S>(rws, v, k);
  }
}

__global__ void __launch_bounds__(384, 2)
fused_block(unsigned char* pws, const u16* __restrict__ pwq, const u16* __restrict__ pkc, const u16* __restrict__ pvc, const u16* __restrict__ px,
            const u16* __restrict__ pc, const u16* __restrict__ pwo, const u16* __restrict__ pw1, const u16* __restrict__ pwm, const u16* __restrict__ pb1,
            const u16* __restrict__ pw2, const u16* __restrict__ pb2, u16* __restrict__ pout
) {
  __shared__ __attribute__((aligned(16))) char smem[163840];
  asm volatile("" ::"v"((LAS char*)smem));
  const int lane = threadIdx.x & 63, wave = __builtin_amdgcn_readfirstlane(threadIdx.x >> 6);
  const int wg = blockIdx.x, x = wg & 7, i = wg >> 3, b = i >> 3, s = i & 7, role = i & 3;
  const bool isM = role == 0, isO = ((i + 1) & 2) != 0, isF = role == 3 && x < 7;
  const int u = x * 8 + (i >> 2);
  const int oc = i >> 1;
  const int cw = wave, g = lane >> 4, c = lane & 15, q4 = (lane >> 2) & 3;
  const u32 kel = 128 * cw + 32 * g + 8 * (c & 3);
  v4u cr, xr;
  u32 xe;
  const u32 em = (lane >> 1) * 6144 + (lane & 1) * 1024 + u * 16, eq = (role * 16 + (lane >> 3)) * 8192 + x * 1024 + (i >> 2) * 128 + (lane & 7) * 16;
  {
    unsigned long long pc_;
    asm volatile("s_getpc_b64 %0\n\ts_mov_b32 m0, %3\n\ts_cmp_lg_u32 %2, 0\n\ts_cbranch_scc1 1f\n\tglobal_load_lds_dword %1, %0\n1:"
                 : "=&s"(pc_) : "v"(min((i * 6 + min(lane, 5)) * 128, CODE_TAIL)), "s"(wave), "i"(L_JUNK) : "memory", "scc");
    *(LAS v4u*)(threadIdx.x * 16) = v4u{0u, 0u, 0u, 0u};
    const u32 mo = (u * 2048) & 0x3fff, f0 = em + mo * 192;
    u32 job = wave < 4 ? (i + 1) & 2 : wave == (isM ? 5 : 4) ? wave : 2, tmp;
    asm volatile("" : "+s"(job));
    asm volatile("s_cmp_eq_u32 %13, 4\n\ts_cbranch_scc0 1f\n\t"
                 "s_mov_b32 m0, %15\n\tv_add_u32 %3, 8*8192-1024, %8\n\tglobal_load_lds_dwordx4 %8, %11 nt\n\tglobal_load_lds_dwordx4 %3, %11 offset:1024 nt\n\t"
                 "v_add_u32 %3, 64*8192-2048, %8\n\tglobal_load_lds_dwordx4 %3, %11 offset:2048 nt\n\tv_add_u32 %3, 72*8192-3072, %8\n\tglobal_load_lds_dwordx4 %3, %11 offset:3072 nt\n\t"
                 "s_mov_b32 m0, %15+4096\n\tv_add_u32 %3, 128*8192, %8\n\tglobal_load_lds_dwordx4 %3, %11 nt\n\tv_add_u32 %3, 136*8192-1024, %8\n\tglobal_load_lds_dwordx4 %3, %11 offset:1024 nt\n\t"
                 "v_add_u32 %3, 192*8192-2048, %8\n\tglobal_load_lds_dwordx4 %3, %11 offset:2048 nt\n\tv_add_u32 %3, 200*8192-3072, %8\n\tglobal_load_lds_dwordx4 %3, %11 offset:3072 nt\n"
                 "1:\n\ts_cmp_eq_u32 %13, 5\n\ts_cbranch_scc0 2f\n\t"
                 "s_mov_b32 m0, %14\n\tv_add_u32 %3, 32*6144-1024, %7\n\tglobal_load_lds_dwordx4 %7, %12\n\tglobal_load_lds_dwordx4 %3, %12 offset:1024\n"
                 "2:\n\ts_cmp_eq_u32 %13, 0\n\ts_cbranch_scc0 3f\n\t"
                 "global_load_dwordx4 %0, %4, %9\n\tglobal_load_dwordx4 %1, %5, %10\n\tglobal_load_dword %2, %6, %10\n3:"
                 : "=&v"(cr), "=&v"(xr), "=&v"(xe), "=&v"(tmp)
                 : "v"(((c >> 2) * 512 + kel) * 2), "v"(threadIdx.x * 16), "v"((g * 512 + u * 8 + (c & 6)) * 2), "v"(f0), "v"(eq), "s"(pc), "s"(px), "s"(pwq), "s"(pwm),
                   "s"(job), "s"(L_T2 + mo), "i"(L_T1)
                 : "memory", "scc");
    asm volatile("" ::"s"(pws), "s"(pkc), "s"(pvc), "s"(pwo), "s"(pw1), "s"(pb1), "s"(pw2), "s"(pb2), "s"(pout));
    wg_barrier();
  }

  if (LIKELY(wave == 4)) {
    __builtin_amdgcn_s_setprio(2);
    const u32 t0 = now();
    flag_set(F_T0, t0 | 1);
    const v4i rq = mkrs(pwq, 512 * 4096 * 2);
    const u32 e0 = eq, e1 = e0 + 8 * 8192 - 1024, e2 = e0 + 64 * 8192 - 2048, e3 = e0 + 72 * 8192 - 3072;
    u32 sched = t0 + KSTART;
    if (isM) {
      flag_wait_hot(F_TI, 1);
      DMA4V("nt", rq, e0, e1, e2, e3, 0, L_T1); DMA4V("nt", rq, e0, e1, e2, e3, 128 * 8192, L_T1 + 4096);
      sched = now() + KSTART;
    }
    sched += s * STAG;
    u32 pace = (int)((t0 + T_END - T_FAST - sched) * 3855u) >> 16, due = sched;
#define PACED() do { u32 n_; while ((int)((n_ = now()) - due) < 0) {} sched += pace; n_ += PACE_MIN; due = (int)(sched - n_) > 0 ? sched : n_; } while (0)
    PACED();
    DMA4V("nt", rq, e0, e1, e2, e3, 256 * 8192, L_T1 + 8192); DMA4V("nt", rq, e0, e1, e2, e3, 384 * 8192, L_T1 + 12288);
    const u32 vo = ((b * 8 + x) * 8192 + 6144 + s * 8) * 256u + (lane >> 4) * 256 + 16 * ((lane & 15) ^ (4 * (lane >> 4))), vp = vo + 16384 - 2048;
    v4i rk = mkrs(pkc, 67108864);
    u32 so = 0, lo = L_K;
#pragma unroll 1
    for (int j = 0; j < 16; j++) {
      PACED();
      DMA4V("nt", rk, vo, vo, vp, vp, so, lo);
      DMA4V("nt", rk, vo, vo, vp, vp, so + 32768, lo + 4096);
      so += 65536; lo += 8192;
      if (!LIKELY(!((0x0a9au >> j) & 1))) {
        if (j == 1) {
          VMWAIT(16); flag_set(F_WQ, 1); STAMP(1);
          if (role == 3) {
            const v4i rm = mkrs(pwm, 512 * 3072 * 2);
            const u32 v0 = (lane >> 2) * 6144 + (lane & 3) * 16 + 2048 + u * 64, v1 = v0 + 16 * 6144 - 1024, v2 = v0 + 32 * 6144 - 2048, v3 = v0 + 48 * 6144 - 3072;
#pragma unroll 1
            for (int n = 0; n < 8; n++) DMA4V("", rm, v0, v1, v2, v3, n * 64 * 6144, L_T2 + n * 4096);
          }
        } else if (j == 3) { if (role == 3) { VMWAIT(16); flag_set(F_T, 1); } }
        else if (j == 4) { const u32 tr = flag_get(F_TR); if (tr) pace = (int)((tr + T_END - sched) * 5958u) >> 16; }
        else if (j == 7) { rk = mkrs(pvc, 67108864); so = 0; }
        else if (j == 9) { VMWAIT(16); flag_set(F_K, 1); STAMP(2); }
        else if (!isO) stage_wait(1, lane);
      }
      VMWAIT(WIN - 8);
    }
    STAMP(3);
    VMWAIT(16); flag_set(F_V, 6); VMWAIT(8); flag_set(F_V, 7); VMWAIT(0); flag_set(F_V, 8);
    STAMP(4);
    return;
  }
  if (LIKELY(wave == 5)) {
    __builtin_amdgcn_s_setprio(3);
    if (isM) {
    const v4i rm = mkrs(pwm, 512 * 3072 * 2);
    const u32 em2 = em + 32 * 6144 - 1024;
    u32 mo = u * 2048 + 2048;
#pragma unroll 1
    for (int n = 7; n > 0; n--) { mo &= 0x3fff; DMA2V("", rm, em, em2, mo * 192, L_T2 + mo); mo += 2048; }
    flag_set(F_TI, 1);
    VMWAIT(0);
    flag_set(F_T, 1);
  }
    flag_wait_hot(F_T0, 1);
    u32 t0 = flag_get(F_T0);
    const rsrc_t rws = __builtin_amdgcn_make_buffer_rsrc(pws, 0, WS_END, 0x00020000);
    if (isO) {
      const u32 o = (oc * 32 + (lane >> 2)) * 6144 + 2048 + x * 512 + (lane & 3) * 128;
      until(t0 + T_RP); TOUCH(o, pwm);
      until(t0 + T_RP + 500); TOUCH(o + 16 * 6144, pwm);
    }
    {
      const u32 tag = load_tag(pws);
      until_idle(t0 + T_S);
      const v2u st = __builtin_amdgcn_raw_buffer_load_b64(rws, WS_ST + (lane & 7) * LS, 0, SC1);
      u32 d = st.y == tag && st.x - t0 < 1600u ? st.x - t0 : 0u;
      d = max(d, dppu<QX1>(d)); d = max(d, dppu<QX2>(d)); d = max(d, dppu<ROR4>(d));
      t0 += __builtin_amdgcn_readfirstlane(d);
      flag_set(F_TR, t0);
    }
    const u32 ts = t0 + s * STAG_E;
    {
      const v4i rw4 = mkrs(pws, WS_END);
      const u32 vh = WS_H + (role * 16 + (lane >> 3)) * LS + (lane & 7) * 16, vh2 = vh + 8 * LS - 1024;
      u32 hn = t0 + T_H;
      until_idle(hn - 200);
      for (u32 spin = 0;; spin++) {
        until(hn);
        if (__all(stage_raw(lane) >= 2u)) break;
        if (spin >= SPIN_CAP) { flag_set(F_GU, 1); break; }
        DMA2V("sc1", rw4, vh, vh2, (spin & 7) * (64 * LS), L_HF);
        VMWAIT(16);
        hn += H_PER;
      }
    }
    __builtin_amdgcn_s_setprio(1);
#define MARKS() do { const u32 tag = flag_get(F_TAG); until_idle(t0 + T_GC); \
      for (u32 spin = 0; spin < SPIN_CAP; spin++) { FENCE(); if (__all(__builtin_amdgcn_raw_buffer_load_b32(rws, WS_GC + (lane & 7) * LS, 0, SC1) == tag)) break; __builtin_amdgcn_s_sleep(4); } \
      flag_set(F_CF, 1); } while (0)
    if (isM) {
      const u32 o0 = (x * 512 + (i >> 2) * 64 + lane) * 1024 + ((3u - (u32)((size_t)pwo >> 7)) & 7u) * 128;
      const u32 o1 = ((i >> 2) * 64 + (lane >> 2)) * 3584 + min(x, 6) * 512 + (lane & 3) * 128;
      MARKS();
      until_idle(t0 + T_LO - 100); TOUCH(o0, pwo);
      until_idle(t0 + T_W); TOUCH(o1, pw1); TOUCH(o1 + 16 * 3584, pw1); TOUCH(o1 + 32 * 3584, pw1); TOUCH(o1 + 48 * 3584, pw1);
      const u32 o2 = (min(x, 6) * 8 + (i >> 2)) * 32768 + lane * 128;
      until_idle(t0 + T_W + W2_GAP); TOUCH(o2, pw2); TOUCH(o2 + 8192, pw2); TOUCH(o2 + 16384, pw2); TOUCH(o2 + 24576, pw2);
    } else if (isO) {
      const v4i rl = mkrs(pwo, 4096 * 512 * 2);
      u32 v0 = ((lane >> 3) + 16 * ((lane >> 2) & 1) + x * 512) * 1024 + oc * 64 + (lane & 3) * 16, v1 = v0 + 7 * 1024, v2 = v0 + 126 * 1024, v3 = v0 + 133 * 1024;
      u32 h0 = (oc & 1) * 16384, h1 = h0 ^ 16384;
      const v4i rw4 = mkrs(pws, WS_END);
      u32 vm = WS_ML + (x * 32 + (lane >> 2)) * 128 + (lane & 3) * 16, vm2 = vm + 1024;
      PIN(v0); PIN(v1); PIN(v2); PIN(v3); PIN(vm); PIN(vm2);
      until_idle(ts + T_LO - 150);
      stage_wait(4, lane);
#define WO8(h) do { DMA4V("", rl, v0, v1, v2, v3, (h) * 4, L_K + (h)); DMA4V("", rl, v0, v1, v2, v3, (h) * 4 + 262144, L_K + 4096 + (h)); } while (0)
      until(ts + T_LO); STAMP(24); WO8(h0);
      until(ts + T_LO + LO_GAP); WO8(h0 + 8192);
      until(ts + T_LO + ML_OFF); DMA2V("sc1", rw4, vm, vm2, 0, L_ML);
      until(ts + T_LO + LO_GAP + LO_GAP2); WO8(h1); WO8(h1 + 8192);
      STAMP(25);
      VMWAIT(0);
      flag_set(F_LATE, 1);
      STAMP(5);
      const u32 tag = flag_get(F_TAG);
      for (u32 spin = 0; !__all((untagged(lds4(L_ML + lane * 16), tag) | untagged(lds4(L_ML + 1024 + lane * 16), tag)) == 0u); spin++) {
        if (spin >= SPIN_CAP) { flag_set(F_GU, 1); break; }
        DMA2V("sc1", rw4, vm, vm2, 0, L_ML);
        VMWAIT(0);
      }
      return;
    } else if (isF) {
      const v4i r2 = mkrs(pw2, 1792 * 512 * 2), r1 = mkrs(pw1, 512 * 1792 * 2);
      u32 w0 = u * 32768 + lane * 16, v0 = (lane >> 2) * 3584 + u * 64 + (lane & 3) * 16, v1 = v0 + 16 * 3584 - 1024, v2 = v0 + 32 * 3584 - 2048, v3 = v0 + 48 * 3584 - 3072;
      MARKS();
      stage_wait(4, lane);
      until_idle(t0 + T_LF);
#pragma unroll 1
      for (int n = 0; n < 8; n++) DMA4V("", r1, v0, v1, v2, v3, n * 64 * 3584, L_K + n * 4096);
      until(t0 + T_LF + LF_GAP);
#pragma unroll 1
      for (int n = 0; n < 8; n++) { DMA4V("", r2, w0, w0, w0, w0, n * 4096, L_K + 32768 + n * 4096); if (n == 1) { VMWAIT(8); flag_set(F_WQ, 2); STAMP(24); } }
      flag_set(F_WQ, 3);
    }
    VMWAIT(0);
    flag_set(F_LATE, 1);
    STAMP(5);
    return;
  }

  const rsrc_t rws = __builtin_amdgcn_make_buffer_rsrc(pws, 0, WS_END, 0x00020000);
  const u32 trow = 128 * cw + 32 * g + q4;
  const u32 rw = ((cw * 4 + g) * 32 + c) * 4;
  u32 tag, modv = 0, b1p = 0;
  v4u xq = {0, 0, 0, 0};
  if (!isO) {
    tag = load_tag(pws);
    asm volatile("s_waitcnt vmcnt(0)" : "+v"(cr), "+v"(xr), "+v"(xe)::"memory");
    v4u sf;
#pragma unroll
    for (int e = 0; e < 4; e++) {
      const float lo = bflo(cr[e]), hi = bfhi(cr[e]);
      sf[e] = pk(lo * __builtin_amdgcn_rcpf(1.f + fexp(-lo)), hi * __builtin_amdgcn_rcpf(1.f + fexp(-hi)));
    }
    v2f w[4] = {unpk(xr.x), unpk(xr.y), unpk(xr.z), unpk(xr.w)};
    const v2f t = (w[0] + w[1]) + (w[2] + w[3]);
    const float mean = wave_sum(t[0] + t[1]) * (1.f / 512.f);
    v2f sq = {0.f, 0.f};
#pragma unroll
    for (int j = 0; j < 4; j++) { const v2f d = w[j] - mean; sq += d * d; }
    const float den = rb(__builtin_amdgcn_sqrtf(rb(rb(wave_sum(sq[0] + sq[1]) * (1.f / 512.f)) + 1e-5f)));
    *(LAS v2f*)(L_LN + 8 * cw) = v2f{rb(mean), __builtin_amdgcn_rcpf(den)};
    PIN(sf);
    if (isM) {
      u32 ta = L_T2 + trow * 32 + 8 * (lane & 3), ho = WS_H + ((lane >> 3) * 64 + ((u >> 1) & 3) * 16 + x * 2 + (u & 1)) * LS + (lane & 7) * 16;
      u32 pa = lane * 16 + cw * 4, hb = ((lane >> 1) & 3) * 64 + (lane & 1) * 16;
      float xv = (c & 1) ? bfhi(xe) : bflo(xe);
      PIN(ta); PIN(ho); PIN(xv); PIN(pa); PIN(hb);
      v4u wf[4];
      for (;;) {
        FENCE();
        const u32 f = flag_raw(F_T);
        FENCE();
#pragma unroll
        for (int k = 0; k < 4; k++) wf[k] = tr2(ta + 256 * k, ta + 256 * k + 128);
        if (__all(f != 0u)) break;
      }
      STAMP0(6);
      *(LAS float*)(L_RM + pa) = dot16(sf, wf);
      flag_set(cw, 1);
      if (cw == 0) {
        v4f p; v2f ln;
        for (;;) {
          FENCE();
          const u32 f = stage_raw(lane);
          FENCE();
          p = __builtin_bit_cast(v4f, lds4v(L_RM + pa)); ln = *(volatile LAS v2f*)(L_LN + 8 * g);
          if (__all(f != 0u)) break;
        }
        const float md = rb((p[0] + p[1]) + (p[2] + p[3]));
        const float h = rb(rb(rb(xv - ln[0]) * ln[1]) * rb(1.f + dppf<ROR8>(md))) + md;
        const u32 hp = pk(h, dppf<QX1>(h));
        st16(rws, v4u{(u32)__builtin_amdgcn_ds_bpermute(hb, hp), tag, (u32)__builtin_amdgcn_ds_bpermute(hb + 8, hp), tag}, ho);
        STAMP(7);
      }
    } else {
      b1p = *(const GAS u32*)(pb1 + min(u, 55) * 32 + 8 * g + 2 * (c & 3));
      xq = *(const GAS v4u*)(px + (c >> 2) * 512 + kel);
      PIN(b1p); PIN(xq);
      flag_wait(F_T, 1);
      v4u wt[8];
      tile32(L_T2 + trow * 64 + 8 * (lane & 3), wt);
      red_put(L_RED + rw, dot32(sf, wt));
      flag_set(cw, 1);
      if (cw == 3) { stage_wait(1, lane); modv = nosent(pk2(sum4(L_RED + lane * 8))); }
    }
  } else {
    const u32 cdue = now() + C_START;
    tag = load_tag(pws);
    const v4u ones = {~0u, ~0u, ~0u, ~0u};
    if (i == 1 && cw == 3) { flag_wait_hot(F_T0, 1); const u32 t = flag_raw(F_T0); if (lane < 8) st16(rws, v4u{t, tag, t, tag}, WS_ST + x * LS + lane * 16); }
    until(cdue);
    st16(rws, ones, (cw < 2 ? WS_O : WS_Y) + (x * 32 + (i & ~1) + (cw & 1)) * 1024 + lane * 16);
    if (cw == 2 && oc < 6) st16(rws, ones, (oc < 4 ? WS_X + (x * 16 + oc * 4) * PS : WS_MODR + (x * 8 + oc * 4 - 16) * PS) + g * PS + c * 16);
    VMWAIT(0);
  }
  u32 qdue;
  {
    v4u wf[4];
    flag_set(F_TAG, tag);
    flag_wait(F_WQ, 1);
#pragma unroll
    for (int k = 0; k < 4; k++) {
      const u32 a = L_T1 + (32 * g + q4) * 128 + cw * 32 + 8 * (lane & 3) + 1024 * k;
      wf[k] = tr2(a, a + 512);
    }
    PIN4(wf);
    u32 qo = WS_Q + (g * 8 + x) * 16384 + b * 4096 + role * 1024 + ((i >> 2) & 1) * 512 + cw * 128 + c * 8;
    u32 ha = (4 * g + (c & 3)) * 128 + (c >> 2) * 32;
    PIN(qo); PIN(ha);
    flag_wait(F_TR, 1);
    qdue = flag_get(F_TR);
    until_idle(qdue + T_H + 500);
    STAMP0(21);
    v4u r0, r1;
    for (u32 spin = 1;; spin++) {
      FENCE();
      r0 = lds4(L_HF + ha); r1 = lds4(L_HF + 16 + ha);
      u32 bad = untagged(r0, tag) | untagged(r1, tag);
      PIN(bad);
      if (__all(bad == 0u)) break;
      if ((spin & 63) == 0 && flag_get(F_GU) != 0u) break;
    }
    STAMP0(8);
    st8(rws, v2u{__float_as_uint(dot16(v4u{r0.x, r0.z, r1.x, r1.z}, wf)), tag}, qo);
    flag_set(cw, 2);
    const u32 q2 = now() + Q_DELAY;
    qdue += T_Q;
    if ((int)(q2 - qdue) > 0) qdue = q2;
    STAMP0(9);
  }
  v4u pf[8], vf[6][2];
  v4f mh;
  bool vearly;
  const float L2E = 1.4426950408889634f;
  const u32 t0 = flag_get(F_TR);
  u32 va = L_V + 256 * (4 * g + q4) + 64 * (cw ^ q4) + 8 * (lane & 3), ob = WS_O + (x * 32 + i) * 1024 + cw * 256 + lane * 4;
  u32 mlo = c < 2 ? WS_ML + (x * 32 + i) * 128 + c * 64 + g * 16 : WS_END;
#define VREAD() do { _Pragma("unroll") for (int k = 0; k < 6; k++) { vf[k][0] = tr2(va + 8192 * k, va + 8192 * k + 4096); vf[k][1] = tr2(va + 8192 * k + 32, va + 8192 * k + 4096 + 32); } } while (0)
  {
    u32 qa = (c >> 2) * QL_S + 16 * g, pa = (c >> 2) * 64 + g * 16;
    u32 pw = pa + (2 * cw + ((c >> 1) & 1)) * 256 + (c & 1) * 8, mw = (c >> 2) * 16 + cw * 4, qw = cw * QL_S + lane * 4;
    u32 qi = WS_Q + (b * 8 + x) * 16384 + cw * 4096 + lane * 16;
    u32 sv = role == 3 ? modv : tag, se = role == 3 && cw == 3 ? WS_MODR + u * PS + lane * 4 : i == 1 && cw == 1 && lane < 32 ? WS_GC + x * LS + lane * 4 : WS_END;
    PIN(va); PIN(ob); PIN(sv); PIN(se);
    const u32 qr = (s & 3) * 1024;
    PIN(qa); PIN(pa); PIN(pw); PIN(mw); PIN(qw); PIN(qi); PIN(mlo);
    until(qdue);
    STAMP0(22);
    v4u kf[4][4];
    {
      v4u r[4];
#pragma unroll
      for (int k = 0; k < 4; k++) r[k] = ld16(rws, qi, (k * 1024) ^ qr);
      flag_wait(F_K, 1);
      const u32 ka = cw * 16384 + 256 * c + 64 * (c & 3) + 16 * g;
#pragma unroll
      for (int j = 0; j < 4; j++)
#pragma unroll
        for (int t = 0; t < 4; t++) kf[j][t] = lds4(L_K + (ka ^ (64 * t)) + 4096 * j);
      flag_set(cw, 4);
      u32 bad = (untagged(r[0], tag) | untagged(r[1], tag)) | (untagged(r[2], tag) | untagged(r[3], tag));
      PIN(bad);
      if (!LIKELY(__all(bad == 0u)))
        for (u32 spin = 0; spin < SPIN_CAP; spin++) {
          FENCE();
#pragma unroll
          for (int k = 0; k < 4; k++)
            if (untagged(r[k], tag) != 0u) r[k] = ld16(rws, qi, (k * 1024) ^ qr);
          if (__all(((untagged(r[0], tag) | untagged(r[1], tag)) | (untagged(r[2], tag) | untagged(r[3], tag))) == 0u)) break;
        }
      *(LAS u32*)(L_QL + qw) = pk((__uint_as_float(r[0].x) + __uint_as_float(r[1].x)) + (__uint_as_float(r[2].x) + __uint_as_float(r[3].x)),
                                  (__uint_as_float(r[0].z) + __uint_as_float(r[1].z)) + (__uint_as_float(r[2].z) + __uint_as_float(r[3].z)));
    }
    flag_set(cw, 5);
    v4u qf[4];
    u32 fv;
    for (;;) {
      const u32 f = stage_raw(lane);
#pragma unroll
      for (int t = 0; t < 4; t++) qf[t] = lds4v(L_QL + qa + 64 * t);
      fv = flag_raw(F_V);
      if (__all(f >= 5u)) break;
    }
    STAMP0(10);
    FENCE();
    v4f a[4], sa;
#pragma unroll
    for (int n = 0; n < 16; n++) {
      const int j = n >> 2, t = n & 3;
      a[j] = t ? mfma(kf[j][t], qf[t], a[j]) : mfma0(kf[j][0], qf[0]);
      if (n < 12) vf[n >> 1][n & 1] = tr2(va + 8192 * (n >> 1) + 32 * (n & 1), va + 8192 * (n >> 1) + 4096 + 32 * (n & 1));
      if (n == 12) { st4(rws, sv, se); sa = (c & 3) == 0 ? a[0] : a[1]; PIN(sa); }
      if (n == 14) { sa = (c & 2) ? a[2] : sa; PIN(sa); }
      SCHED();
    }
    vearly = __builtin_amdgcn_readfirstlane(fv) >= 6u;
    sa = (c & 3) == 3 ? a[3] : sa;
    const u32 sp0 = pk(sa[0], sa[1]), sp1 = pk(sa[2], sa[3]);
    const float SC = 0.08838834764831845f;
    const v2f s0 = rb2(unpk(sp0) * SC), s1 = rb2(unpk(sp1) * SC);
    const float m = qg_max(s0[0], s0[1], s1[0], s1[1]);
    const v2f e0 = (s0 - m) * L2E, e1 = (s1 - m) * L2E;
    const float p0 = __builtin_amdgcn_exp2f(e0[0]), p1 = __builtin_amdgcn_exp2f(e0[1]), p2 = __builtin_amdgcn_exp2f(e1[0]), p3 = __builtin_amdgcn_exp2f(e1[1]);
    *(LAS v2u*)(L_PL + pw) = v2u{pk(p0, p1), pk(p2, p3)};
    *(LAS float*)(L_MX + mw) = m;
    flag_set(cw, 6);
    SCHED();
    *(LAS float*)(L_SM + mw) = xg_sum(quad_sum((p0 + p1) + (p2 + p3)));
    flag_set(cw, 7);
    for (;;) {
      const u32 f = stage_raw(lane);
#pragma unroll
      for (int k = 0; k < 8; k++) pf[k] = lds4v(L_PL + pa + k * 256);
      mh = __builtin_bit_cast(v4f, lds4v(L_MX + g * 16));
      if (__all(f >= 6u)) break;
    }
    STAMP0(12);
  }
  {
    float fs[4], me;
#define SCALES() do { const float mo = fmx(fmx(mh[0], mh[1]), fmx(mh[2], mh[3])); me = __builtin_ceilf(mo * L2E); const float fr = mo * L2E - me; \
      _Pragma("unroll") for (int w = 0; w < 4; w++) fs[w] = __builtin_amdgcn_exp2f((mh[w] - mo) * L2E + fr); \
      if (cw == 0) { stage_wait(7, lane); const v4f sm = *(LAS v4f*)(L_SM + g * 16); \
        st16(rws, v4u{__float_as_uint(me), tag, __float_as_uint((sm[0] * fs[0] + sm[1] * fs[1]) + (sm[2] * fs[2] + sm[3] * fs[3])), tag}, mlo); } } while (0)
    if (!vearly) { SCALES(); flag_wait(F_V, 6); VREAD(); }
    SCHED();
    v4f d[3][2];
#pragma unroll
    for (int w = 0; w < 3; w++)
#pragma unroll
      for (int n = 0; n < 2; n++) d[w][n] = mfma(pf[2 * w + 1], vf[2 * w + 1][n], mfma0(pf[2 * w], vf[2 * w][n]));
    SCHED();
    if (vearly) SCALES();
    float oa = d[0][0][0] * fs[0] + d[1][0][0] * fs[1] + d[2][0][0] * fs[2], oz = d[0][1][0] * fs[0] + d[1][1][0] * fs[1] + d[2][1][0] * fs[2];
    PIN(oa); PIN(oz);
    v4f o0, o1;
#pragma unroll
    for (int k = 6; k < 8; k++) {
      v4u v0, v1;
      for (;;) {
        FENCE();
        const u32 f = flag_raw(F_V);
        FENCE();
        v0 = tr2(va + 8192 * k, va + 8192 * k + 4096); v1 = tr2(va + 8192 * k + 32, va + 8192 * k + 4096 + 32);
        if (__all(f > (u32)k)) break;
      }
      if (k == 6) { o0 = mfma0(pf[6], v0); o1 = mfma0(pf[6], v1); SCHED(); } else { o0 = mfma(pf[7], v0, o0); o1 = mfma(pf[7], v1, o1); }
    }
    st4(rws, nosent(pk(fmaf(o0[0], fs[3], oa), fmaf(o1[0], fs[3], oz))), ob);
  }
  const u32 tpub = now();
  STAMP0(13);
  const u32 hm = (c & 1) ? 0xffff0000u : 0x0000ffffu, mk0 = (c & 2) ? 0u : hm, mk1 = (c & 2) ? hm : 0u;
  if (isO) {
    u32 og = WS_O + (x * 32 + (c >> 2) * 8) * 1024 + cw * 256 + g * 64 + (c & 3) * 16, mi = c * 128 + g * 16, xo = WS_X + (x * 16 + oc) * PS + lane * 4;
    u32 ta = L_K + trow * 64 + 8 * (lane & 3), rp = L_RED + rw, rg = L_RED + lane * 8;
    PIN(og); PIN(mi); PIN(xo); PIN(ta); PIN(rp); PIN(rg);
    until(t0 + T_G);
    until(tpub + G_MIN);
    v4u r[8], wt[8], m0, m1;
#pragma unroll
    for (int k = 0; k < 8; k++) r[k] = gld<1024>(rws, og, k);
    for (u32 spin = 1;; spin++) {
      FENCE();
      m0 = lds4(L_ML + mi); m1 = lds4(L_ML + 64 + mi);
      u32 bad = untagged(m0, tag) | untagged(m1, tag);
      PIN(bad);
      if (__all(bad == 0u)) break;
      if ((spin & 63) == 0 && flag_get(F_GU) != 0u) break;
    }
    float M = fmx(__uint_as_float(m0.x), __uint_as_float(m1.x));
    M = fmx(M, dppf<QX1>(M)); M = fmx(M, dppf<QX2>(M));
    const float w0 = __builtin_amdgcn_exp2f(__uint_as_float(m0.x) - M), w1 = __builtin_amdgcn_exp2f(__uint_as_float(m1.x) - M);
    float linv = __builtin_amdgcn_rcpf(quad_sum(w0 * __uint_as_float(m0.z) + w1 * __uint_as_float(m1.z)));
    const u32 p0 = pk(w0, w0), p1 = pk(w1, w1);
    v2u w[8] = {v2u{dppu<0x00>(p0) & mk0, dppu<0x00>(p0) & mk1}, v2u{dppu<0x00>(p1) & mk0, dppu<0x00>(p1) & mk1}, v2u{dppu<0x55>(p0) & mk0, dppu<0x55>(p0) & mk1},
                v2u{dppu<0x55>(p1) & mk0, dppu<0x55>(p1) & mk1}, v2u{dppu<0xAA>(p0) & mk0, dppu<0xAA>(p0) & mk1}, v2u{dppu<0xAA>(p1) & mk0, dppu<0xAA>(p1) & mk1},
                v2u{dppu<0xFF>(p0) & mk0, dppu<0xFF>(p0) & mk1}, v2u{dppu<0xFF>(p1) & mk0, dppu<0xFF>(p1) & mk1}};
    PIN8(w); PIN(linv);
    STAMP0(11);
    const u32 fl = flag_raw(F_LATE);
    FENCE();
    tile32(ta, wt);
    SCHED();
    v4f a0, a1;
    wsum8(r, w, a0, a1);
    if (!LIKELY(!any_nan(a0[0], a1[0]))) { reread8<1024>(rws, og, r); wsum8(r, w, a0, a1); }
    STAMP0(16);
    a0 *= linv; a1 *= linv;
    v4u af = {pk(a0[0], a0[1]), pk(a0[2], a0[3]), pk(a1[0], a1[1]), pk(a1[2], a1[3])};
    if (__builtin_amdgcn_readfirstlane(fl) == 0u) {
      PIN(af);
      for (;;) {
        FENCE();
        const u32 f = flag_raw(F_LATE);
        FENCE();
        tile32(ta, wt);
        if (__all(f != 0u)) break;
      }
    }
    red_put(rp, dot32(af, wt));
    wg_barrier();
    if (cw == 0) { st4(rws, nosent(pk2(sum4(rg))), xo); STAMP(17); }
  } else if (isM) {
    if (cw == 1) {
      const int ub = u >> 4, ct = u & 15, el = 8 * (c & 3) + 4 * (g & 1);
      const v2u xv = *(const GAS v2u*)(px + ub * 512 + ct * 32 + el), b2v = *(const GAS v2u*)(pb2 + ct * 32 + el);
      const u32 one = 0x3F803F80u & hm;
      v4u sl = {(c >> 1) == 0 ? one : 0u, (c >> 1) == 1 ? one : 0u, (c >> 1) == 2 ? one : 0u, (c >> 1) == 3 ? one : 0u};
      flag_wait(F_CF, 1);
      v2u gav, gbv;
      for (u32 spin = 0; spin < SPIN_CAP; spin++) {
        FENCE();
        gav = __builtin_amdgcn_raw_buffer_load_b64(rws, WS_MODR + ct * PS + ub * 64 + el * 2, 0, SC1);
        gbv = __builtin_amdgcn_raw_buffer_load_b64(rws, WS_MODR + (48 + ct) * PS + ub * 64 + el * 2, 0, SC1);
        if (__all(max(max(gav.x, gav.y), max(gbv.x, gbv.y)) != 0xffffffffu)) break;
      }
      const u32 xi = lane < 32 ? WS_X + ((lane >> 2) * 16 + ct) * PS + ub * 64 + (lane & 3) * 16 : WS_END;
      until_idle(t0 + T_X);
      v4u xp;
      for (u32 spin = 0; spin < SPIN_CAP; spin++) { FENCE(); xp = ld16(rws, xi, 0); if (!__any(unset(xp))) break; }
      const u32 ydue = now() + Y_DELAY;
      v4f at = mfma0(sl, xp);
#pragma unroll
      for (int r = 0; r < 4; r++) { at[r] += dppf<ROR4>(at[r]); at[r] += dppf<ROR8>(at[r]); }
      v2f x10 = rb2(unpk(xv.x) + rb2(unpk(gav.x) * rb2(v2f{at[0], at[1]}))), x11 = rb2(unpk(xv.y) + rb2(unpk(gav.y) * rb2(v2f{at[2], at[3]})));
      v2f g0 = unpk(gbv.x), g1 = unpk(gbv.y);
      x10 += g0 * unpk(b2v.x); x11 += g1 * unpk(b2v.y);
      u32 yi = WS_Y + (u >> 1) * 8192 + (lane >> 2) * 128 + (u & 1) * 64 + (lane & 3) * 16, y3 = lane < 32 ? yi + 6144 : WS_END;
      u32 oo = lane < 32 && c < 4 ? (ub * 512 + ct * 32 + el) * 2 : 4096;
      const rsrc_t ro = __builtin_amdgcn_make_buffer_rsrc(pout, 0, 4096, 0x00020000);
      PIN(x10); PIN(x11); PIN(g0); PIN(g1); PIN(yi); PIN(y3); PIN(oo); PIN(sl);
      STAMP(18);
      until(ydue);
      until(t0 + T_X + Y_OFF);
      v4f ys;
      for (u32 spin = 0;; spin++) {
        FENCE();
        const v4u y0 = ld16(rws, yi, 0), y1 = ld16(rws, yi + 2048, 0), y2 = ld16(rws, yi, 4096), y4 = ld16(rws, y3, 0);
        ys = mfma(sl, y4, mfma(sl, y2, mfma(sl, y1, mfma0(sl, y0))));
        if (LIKELY(!any_nan(ys[0], ys[0]))) break;
        if (!__any(unset(y0) | unset(y1) | unset(y2) | unset(y4)) || spin >= SPIN_CAP) break;
      }
      STAMP(19);
#pragma unroll
      for (int r = 0; r < 4; r++) ys[r] += dppf<ROR4>(ys[r]);
#pragma unroll
      for (int r = 0; r < 4; r++) ys[r] += dppf<ROR8>(ys[r]);
      __builtin_amdgcn_raw_buffer_store_b64(v2u{pk2(x10 + g0 * v2f{ys[0], ys[1]}), pk2(x11 + g1 * v2f{ys[2], ys[3]})}, ro, oo, 0, 0);
      if (u == 0 && lane == 0) st4(rws, tag, WS_EPOCH);
      STAMP(20);
    }
  } else if (isF) {
    flag_wait(F_CF, 1);
    const u32 po = (4 * cw + g) * PS + c * 16;
    v4u gq, sf, cq;
    for (u32 spin = 0; spin < SPIN_CAP; spin++) {
      FENCE();
      gq = ld16(rws, WS_MODR + po, 0); sf = ld16(rws, WS_MODR + po, 16 * PS); cq = ld16(rws, WS_MODR + po, 32 * PS);
      if (!__any(unset(gq) | unset(sf) | unset(cq))) break;
    }
    const float piv = *(LAS float*)(L_LN + (c >> 2) * 8);
    v4u gf;
    v2f ga[4], xz[4], gg[4], xg[4];
#pragma unroll
    for (int e = 0; e < 4; e++) {
      gf[e] = pk2(unpk(cq[e]) + 1.f);
      ga[e] = unpk(gq[e]); xz[e] = unpk(xq[e]) - piv; gg[e] = ga[e] * unpk(gf[e]); xg[e] = xz[e] * unpk(gf[e]);
    }
    u32 ra = ((c >> 2) * 32 + 8 * g + 2 * (c & 3)) * 4, rp = rw, sp = c * 32, sw = (c >> 2) * 128 + cw * 32 + g * 8, ta = L_K + trow * 64 + 8 * (lane & 3), xi = WS_X + po;
    u32 yo = WS_Y + (g * 8 + 2 * cw + (c >> 3)) * 8192 + u * 128 + (c & 7) * 16;
    v2u id = {0x3F803F80u & mk0, 0x3F803F80u & mk1};
    PIN4(ga); PIN4(xz); PIN4(gg); PIN4(xg); PIN(gf); PIN(sf); PIN(ra); PIN(rp); PIN(sp); PIN(sw); PIN(xi); PIN(yo); PIN(id); PIN(ta);
    u32 rq = L_RED + rw;
    PIN(rq);
    v2f b1v = unpk(b1p);
    PIN(b1v);
    STAMP0(28);
    v4u r[8], wt[8], w2f[8];
#define XREAD() do { flag_wait_hot(F_WQ, 3); until(t0 + T_X); _Pragma("unroll") for (int k = 0; k < 8; k++) r[k] = gld<16 * PS>(rws, xi, k); } while (0)
    until_idle(t0 + T_LF + 400);
    flag_wait_hot(F_WQ, 2);
    tile32(ta, wt);
    const bool first = (int)(now() - (t0 + T_X - X_PREP)) > 0;
    if (first) XREAD();
    red_put(L_TS + rp, dot32(gf, wt));
    red_put(L_TS + 2048 + rp, dot32(sf, wt));
    flag_set(cw, 8);
    STAMP0(18);
    if (!first) XREAD();
    stage_wait(8, lane);
    v2f T = sum4(L_TS + ra), S = sum4(L_TS + 2048 + ra) + b1v;
    PIN(T); PIN(S);
    flag_wait_hot(F_LATE, 1);
    {
      v4u w2r[8];
#pragma unroll
      for (int k = 0; k < 8; k++) w2r[k] = lds4(L_K + 32768 + (8 * g + k) * 1024 + cw * 256 + c * 16);
#pragma unroll
      for (int j = 0; j < 8; j++)
#pragma unroll
        for (int e = 0; e < 4; e++) w2f[j][e] = __builtin_amdgcn_perm(w2r[2 * e + 1][j >> 1], w2r[2 * e][j >> 1], (j & 1) ? 0x07060302u : 0x05040100u);
      PIN8(w2f);
    }
    STAMP0(15);
    const v2u w[8] = {id, id, id, id, id, id, id, id};
    v4f a0, a1;
    wsum8(r, w, a0, a1);
    if (!LIKELY(!any_nan(a0[0], a1[0]))) { reread8<16 * PS>(rws, xi, r); wsum8(r, w, a0, a1); }
    STAMP0(16);
    const v2f at[4] = {v2f{a0[0], a0[1]}, v2f{a0[2], a0[3]}, v2f{a1[0], a1[1]}, v2f{a1[2], a1[3]}};
    const v4u af = {pk2(at[0] * gg[0] + xg[0]), pk2(at[1] * gg[1] + xg[1]), pk2(at[2] * gg[2] + xg[2]), pk2(at[3] * gg[3] + xg[3])};
    SCHED();
    v4f d[8];
    v2f z[4], s1, s2;
    float q1, q2;
    d[0] = mfma0(af, wt[0]); z[0] = at[0] * ga[0] + xz[0]; z[1] = at[1] * ga[1] + xz[1]; SCHED();
    d[1] = mfma0(af, wt[1]); z[2] = at[2] * ga[2] + xz[2]; z[3] = at[3] * ga[3] + xz[3]; SCHED();
    d[2] = mfma0(af, wt[2]); s1 = z[0] + z[1]; s2 = z[0] * z[0]; SCHED();
    d[3] = mfma0(af, wt[3]); s1 += z[2]; s2 += z[1] * z[1]; SCHED();
    d[4] = mfma0(af, wt[4]); s1 += z[3]; s2 += z[2] * z[2]; SCHED();
    d[5] = mfma0(af, wt[5]); q1 = s1[0] + s1[1]; PIN(q1); s2 += z[3] * z[3]; SCHED();
    d[6] = mfma0(af, wt[6]); q2 = s2[0] + s2[1]; PIN(q2); q1 += dppf<QX1>(q1); PIN(q1); SCHED();
    d[7] = mfma0(af, wt[7]); SCHED(); q2 += dppf<QX1>(q2); PIN(q2); q1 += dppf<QX2>(q1); PIN(q1); SCHED();
    float p0 = d[0][0] + d[2][1], p1 = d[1][0] + d[3][1];
    q2 += dppf<QX2>(q2);
    PIN(p0); PIN(p1);
    *(LAS v2f*)(L_ST + sw) = v2f{q1, q2};
    p0 += d[4][2]; p1 += d[5][2];
    PIN(p0); PIN(p1);
    p0 += d[6][3]; p1 += d[7][3];
    asm volatile("" ::"v"(d[0]), "v"(d[1]), "v"(d[2]), "v"(d[3]), "v"(d[4]), "v"(d[5]), "v"(d[6]), "v"(d[7]), "v"(p0), "v"(p1));
    red_put(rq, v2f{p0, p1});
    wg_barrier();
    const v4f st0 = *(LAS v4f*)(L_ST + sp), st1 = *(LAS v4f*)(L_ST + 16 + sp);
    const v2f P = sum4(L_RED + ra);
    v2f st = (v2f{st0[0], st0[1]} + v2f{st0[2], st0[3]}) + (v2f{st1[0], st1[1]} + v2f{st1[2], st1[3]});
    { float u0 = st[0], u1 = st[1]; u0 += dppf<QX1>(u0); PIN(u0); u1 += dppf<QX1>(u1); PIN(u1); u0 += dppf<QX2>(u0); PIN(u0); u1 += dppf<QX2>(u1); st = v2f{u0, u1} * (1.f / 512.f); }
    const float rs = __builtin_amdgcn_rsqf(fmx(st[1] - st[0] * st[0], 0.f) + 1e-5f);
    const v2f pre = (P - T * st[0]) * rs + S;
    const float h0 = pre[0] > 0.f ? pre[0] : 0.f, h1 = pre[1] > 0.f ? pre[1] : 0.f;
    const u32 hp = pk(h0 * h0, h1 * h1);
    struct { u32 x, y, z, w; } yv;
    const v4u hf = {dppu<0x00>(hp), dppu<0x55>(hp), dppu<0xAA>(hp), dppu<0xFF>(hp)};
#pragma unroll
    for (int j = 0; j < 8; j++) {
      d[j] = mfma0(hf, w2f[j]);
    }
    SCHED();
    yv.x = nosent(pk(d[0][0], d[1][0])); SCHED(); yv.y = nosent(pk(d[2][0], d[3][0])); SCHED(); yv.z = nosent(pk(d[4][0], d[5][0])); SCHED(); yv.w = nosent(pk(d[6][0], d[7][0]));
    asm volatile("" ::"v"(d[0]), "v"(d[1]), "v"(d[2]), "v"(d[3]), "v"(d[4]), "v"(d[5]), "v"(d[6]), "v"(d[7]), "v"(yv.w));
    st16(rws, v4u{yv.x, yv.y, yv.z, yv.w}, yo);
    STAMP0(17);
  }
}

extern "C" void kernel_launch(void* const* d_in, const int* in_sizes, int n_in, void* d_out, int out_size, void* d_ws, size_t ws_size, hipStream_t stream) {
  fused_block<<<256, 384, 0, stream>>>((unsigned char*)d_ws, (const u16*)d_in[3], (const u16*)d_in[1], (const u16*)d_in[2], (const u16*)d_in[0], (const u16*)d_in[10],
                                       (const u16*)d_in[4], (const u16*)d_in[6], (const u16*)d_in[11], (const u16*)d_in[7], (const u16*)d_in[8], (const u16*)d_in[9],
                                       (u16*)d_out
  );
}
```
